```python
import torch

def _softmax(x, axis=-1):
    return torch.softmax(x, dim=axis)

def _gelu(x):
    return torch.nn.functional.gelu(x, approximate='tanh')

def _silu(x):
    return torch.nn.functional.silu(x)

def _sigmoid(x):
    return torch.sigmoid(x)

def _layernorm(x, axis=-1, eps=1e-05):
    m = x.mean(dim=axis, keepdim=True)
    v = x.var(dim=axis, keepdim=True, unbiased=False)
    return (x - m) / torch.sqrt(v + eps)

def _scatter_add(tgt, idx, src, axis=0):
    return tgt.clone().index_add_(axis, idx, src)

def _segment_sum(src, seg, n):
    out = torch.zeros((n,) + src.shape[1:], dtype=src.dtype)
    return out.index_add_(0, seg, src)

def _pad(x, pw):
    flat = tuple((v for pair in reversed(pw) for v in pair))
    return torch.nn.functional.pad(x, flat)
kernel_name = 'adaLN_cond__gqa_block_cached__dense_activation_silu__decode'

def setup_inputs():

    _w = lambda a: (lambda t: t.to(torch.bfloat16) if t.is_floating_point() else t)(torch.as_tensor(a))

    def _A_setup():
        B, Sq, D = (1, 1, 512)
        Hq, Hkv, Sk, Dh = (32, 8, 65536, 128)
        assert Hq % Hkv == 0, (Hq, Hkv)
        return {'x': _w((lambda *s: torch.randn(*s, dtype=torch.bfloat16) * 0.1)(B, Sq, D)), 'k_cache': _w((lambda *s: torch.randn(*s, dtype=torch.bfloat16) * 0.8)(B, Hkv, Sk, Dh)), 'v_cache': _w((lambda *s: torch.randn(*s, dtype=torch.bfloat16) * 3.2)(B, Hkv, Sk, Dh)), 'wq': _w((lambda *s: torch.randn(*s, dtype=torch.bfloat16) * 0.8)(D, Hq * Dh) * D ** (-0.5)), 'wo': _w((lambda *s: torch.randn(*s, dtype=torch.bfloat16) * 3.2)(Hq * Dh, D) * (Hq * Dh) ** (-0.5))}
    _A_in = _A_setup()

    def _B_setup():
        B, S, D, F = (1, 1, 512, 1792)
        return {'x': _w((lambda *s: torch.randn(*s, dtype=torch.bfloat16) * 0.1)(B, S, D)), 'w1': _w((lambda *s: torch.randn(*s, dtype=torch.bfloat16) * 12.8)(D, F) * D ** (-0.5)), 'w2': _w((lambda *s: torch.randn(*s, dtype=torch.bfloat16) * 12.8)(F, D) * F ** (-0.5))}
    _B_in = _B_setup()
    return {'A_x': _A_in['x'], 'A_k_cache': _A_in['k_cache'], 'A_v_cache': _A_in['v_cache'], 'A_wq': _A_in['wq'], 'A_wo': _A_in['wo'], 'B_x': _B_in['x'], 'B_w1': _B_in['w1'], 'B_w2': _B_in['w2'], 'c': _w((lambda *s: torch.randn(*s, dtype=torch.bfloat16) * 0.2)(1, 512) * 8.0), 'w_mod': _w((lambda *s: torch.randn(*s, dtype=torch.bfloat16) * 0.2)(512, 6 * 512) * 512 ** (-0.5) * 8.0)}

def reference(A_x, A_k_cache, A_v_cache, A_wq, A_wo, B_x, B_w1, B_w2, c, w_mod):

    def _A_ref(x, k_cache, v_cache, wq, wo):
        B, Sq, D = x.shape
        Hq, Dh = (32, 128)
        Hkv = k_cache.shape[1]
        rep = Hq // Hkv
        q = (lambda x, *d: x.permute(*d))((x @ wq).reshape(B, Sq, Hq, Dh), 0, 2, 1, 3)
        k = (lambda x, n, axis: torch.repeat_interleave(x, n, dim=axis))(k_cache, rep, axis=1)
        v = (lambda x, n, axis: torch.repeat_interleave(x, n, dim=axis))(v_cache, rep, axis=1)
        scores = torch.einsum('bhqd,bhkd->bhqk', q, k) * Dh ** (-0.5)
        p = _softmax(scores, axis=-1)
        o = torch.einsum('bhqk,bhkd->bhqd', p, v)
        return (lambda x, *d: x.permute(*d))(o, 0, 2, 1, 3).reshape(B, Sq, Hq * Dh) @ wo

    def _B_ref(x, w1, w2):
        h = torch.einsum('bsd,df->bsf', x, w1)
        h = _silu(h)
        out = torch.einsum('bsf,fd->bsd', h, w2)
        return out
    x = A_x
    mod = (_silu(c) @ w_mod).reshape(c.shape[0], 6, 1, -1)
    sa, ca, ga, sb, cb, gb = (mod[:, 0].clone(), mod[:, 1].clone(), mod[:, 2].clone(), mod[:, 3].clone(), mod[:, 4].clone(), mod[:, 5].clone())
    h = _layernorm(x, axis=-1) * (1 + ca) + sa
    x = x + ga * _A_ref(h, A_k_cache, A_v_cache, A_wq, A_wo)
    h = _layernorm(x, axis=-1) * (1 + cb) + sb
    x = x + gb * _B_ref(h, B_w1, B_w2)
    return x
```

```cpp
#include <hip/hip_runtime.h>
#include <hip/hip_bf16.h>
#include <stdint.h>

#define GAS __attribute__((address_space(1)))
#define LAS __attribute__((address_space(3)))
typedef unsigned v4u __attribute__((ext_vector_type(4)));
typedef float f32x4 __attribute__((ext_vector_type(4)));
typedef short bf16x8 __attribute__((ext_vector_type(8)));
typedef unsigned short u16;

constexpr int D = 512, HQ = 32, HKV = 8, SK = 65536, DH = 128, FF = 1792;
constexpr int NSPLIT = 32;

__device__ __forceinline__ float bf2f(unsigned b) { return __builtin_bit_cast(float, b << 16); }
__device__ __forceinline__ float lo16(unsigned w) { return __builtin_bit_cast(float, w << 16); }
__device__ __forceinline__ float hi16(unsigned w) { return __builtin_bit_cast(float, w & 0xffff0000u); }
__device__ __forceinline__ unsigned cvtpk(float lo, float hi) {
  typedef __bf16 bf16x2 __attribute__((ext_vector_type(2)));
  bf16x2 r; r.x = (__bf16)lo; r.y = (__bf16)hi;
  return __builtin_bit_cast(unsigned, r);
}
__device__ __forceinline__ float rbf(float x) { return (float)(__bf16)x; }
__device__ __forceinline__ f32x4 mfma(v4u a, v4u b, f32x4 c) {
  return __builtin_amdgcn_mfma_f32_16x16x32_bf16(__builtin_bit_cast(bf16x8, a), __builtin_bit_cast(bf16x8, b), c, 0, 0, 0);
}
__device__ __forceinline__ float silu(float x) { return x / (1.0f + __expf(-x)); }

template <int K, int N>
__device__ __forceinline__ float gemv64(const float* a_lds, const u16* W, int n0, float* red) {
  const int tid = threadIdx.x, cc = tid & 7, rg = tid >> 3;
  float acc[8];
#pragma unroll
  for (int i = 0; i < 8; ++i) acc[i] = 0.f;
  for (int k = rg; k < K; k += 32) {
    const v4u w = *(const v4u*)(W + (size_t)k * N + n0 + cc * 8);
    const float a = a_lds[k];
#pragma unroll
    for (int i = 0; i < 4; ++i) { acc[2 * i] += a * lo16(w[i]); acc[2 * i + 1] += a * hi16(w[i]); }
  }
#pragma unroll
  for (int i = 0; i < 8; ++i) red[rg * 64 + cc * 8 + i] = acc[i];
  __syncthreads();
  float s = 0.f;
  if (tid < 64) for (int r = 0; r < 32; ++r) s += red[r * 64 + tid];
  return s;
}

__global__ void __launch_bounds__(256) k_mod(const u16* c, const u16* wmod, float* mod) {
  __shared__ float a[D]; __shared__ float red[32 * 64];
  for (int i = threadIdx.x; i < D; i += 256) a[i] = rbf(silu(bf2f(c[i])));
  __syncthreads();
  const float s = gemv64<D, 6 * D>(a, wmod, blockIdx.x * 64, red);
  if (threadIdx.x < 64) mod[blockIdx.x * 64 + threadIdx.x] = rbf(s);
}

__device__ __forceinline__ void ln_mod(const float* x, const float* sh, const float* sc, float* h, float* red) {
  const int tid = threadIdx.x;
  float s = 0.f;
  for (int i = tid; i < D; i += 256) s += x[i];
  for (int o = 32; o >= 1; o >>= 1) s += __shfl_xor(s, o);
  if ((tid & 63) == 0) red[tid >> 6] = s;
  __syncthreads();
  const float mean = (red[0] + red[1] + red[2] + red[3]) * (1.0f / D);
  __syncthreads();
  float v = 0.f;
  for (int i = tid; i < D; i += 256) { const float d = x[i] - mean; v += d * d; }
  for (int o = 32; o >= 1; o >>= 1) v += __shfl_xor(v, o);
  if ((tid & 63) == 0) red[tid >> 6] = v;
  __syncthreads();
  const float var = rbf((red[0] + red[1] + red[2] + red[3]) * (1.0f / D));
  const float mb = rbf(mean);
  const float sd = rbf(sqrtf(rbf(var + 1e-5f)));
  for (int i = tid; i < D; i += 256) {
    const float n = rbf(rbf(x[i] - mb) / sd);
    h[i] = rbf(rbf(n * rbf(1.0f + sc[i])) + sh[i]);
  }
  __syncthreads();
}

__global__ void __launch_bounds__(256) k_q(const u16* x, const float* mod, const u16* wq, u16* q) {
  __shared__ float xs[D]; __shared__ float h[D]; __shared__ float red[32 * 64];
  for (int i = threadIdx.x; i < D; i += 256) xs[i] = bf2f(x[i]);
  __syncthreads();
  ln_mod(xs, mod, mod + D, h, red);
  const float s = gemv64<D, HQ * DH>(h, wq, blockIdx.x * 64, red);
  if (threadIdx.x < 64) q[blockIdx.x * 64 + threadIdx.x] = (u16)(cvtpk(s, 0.f) & 0xffffu);
}

__device__ __forceinline__ float xmax16_32(float x) {
  auto s = __builtin_amdgcn_permlane16_swap(__float_as_uint(x), __float_as_uint(x), false, false);
  x = fmaxf(__uint_as_float(s[0]), __uint_as_float(s[1]));
  auto t = __builtin_amdgcn_permlane32_swap(__float_as_uint(x), __float_as_uint(x), false, false);
  return fmaxf(__uint_as_float(t[0]), __uint_as_float(t[1]));
}
__device__ __forceinline__ float xsum16_32(float x) {
  auto s = __builtin_amdgcn_permlane16_swap(__float_as_uint(x), __float_as_uint(x), false, false);
  x = __uint_as_float(s[0]) + __uint_as_float(s[1]);
  auto t = __builtin_amdgcn_permlane32_swap(__float_as_uint(x), __float_as_uint(x), false, false);
  return __uint_as_float(t[0]) + __uint_as_float(t[1]);
}
template <int CTRL> __device__ __forceinline__ float dpp(float x) {
  return __builtin_bit_cast(float, __builtin_amdgcn_mov_dpp(__builtin_bit_cast(int, x), CTRL, 0xf, 0xf, true));
}

__global__ void __launch_bounds__(512, 2) k_attn(const u16* kc, const u16* vc, const u16* q, float* part) {
  __shared__ float sm[8][4][DH + 4];
  const int tid = threadIdx.x, lane = tid & 63, w = __builtin_amdgcn_readfirstlane(tid >> 6);
  const int g = lane >> 4, n = lane & 15;
  const int kvh = blockIdx.x & 7, sp = blockIdx.x >> 3;
  constexpr float C2 = 0.088388347648318440f * 1.4426950408889634f;
  v4u qf[4];
#pragma unroll
  for (int s = 0; s < 4; ++s) qf[s] = *(const v4u*)(q + (kvh * 4 + (n & 3)) * DH + 32 * s + 8 * g);
  const int krow = 4 * (n & 3) + (n >> 2);
  const GAS unsigned char* kb = (const GAS unsigned char*)kc + ((size_t)kvh * SK + krow) * 256 + 16 * g;
  const GAS unsigned char* vb = (const GAS unsigned char*)vc + ((size_t)kvh * SK + g) * 256 + 16 * n;
  f32x4 acc[8];
#pragma unroll
  for (int e = 0; e < 8; ++e) acc[e] = f32x4{0.f, 0.f, 0.f, 0.f};
  float m = -INFINITY, l = 0.f;
  constexpr int NT = SK / NSPLIT / 32 / 8;
  for (int it = 0; it < NT; ++it) {
    const size_t key0 = (size_t)sp * (SK / NSPLIT) + (size_t)(it * 8 + w) * 32;
    v4u kf[2][4], vr[8];
#pragma unroll
    for (int t = 0; t < 2; ++t)
#pragma unroll
      for (int s = 0; s < 4; ++s) kf[t][s] = __builtin_nontemporal_load((const GAS v4u*)(kb + (key0 + 16 * t) * 256 + 64 * s));
#pragma unroll
    for (int j = 0; j < 8; ++j) vr[j] = __builtin_nontemporal_load((const GAS v4u*)(vb + (key0 + 4 * j) * 256));
    f32x4 sc[2];
#pragma unroll
    for (int t = 0; t < 2; ++t) {
      sc[t] = f32x4{0.f, 0.f, 0.f, 0.f};
#pragma unroll
      for (int s = 0; s < 4; ++s) sc[t] = mfma(kf[t][s], qf[s], sc[t]);
    }
    float tm = fmaxf(fmaxf(fmaxf(sc[0][0], sc[0][1]), fmaxf(sc[0][2], sc[0][3])), fmaxf(fmaxf(sc[1][0], sc[1][1]), fmaxf(sc[1][2], sc[1][3])));
    tm = xmax16_32(tm);
    const float mn = fmaxf(m, tm * C2);
    const float alpha = __builtin_amdgcn_exp2f(m - mn);
    m = mn;
    float p[8];
#pragma unroll
    for (int j = 0; j < 8; ++j) p[j] = __builtin_amdgcn_exp2f(sc[j >> 2][j & 3] * C2 - mn);
    l = l * alpha + (((p[0] + p[1]) + (p[2] + p[3])) + ((p[4] + p[5]) + (p[6] + p[7])));
    const v4u pf = {cvtpk(p[0], p[1]), cvtpk(p[2], p[3]), cvtpk(p[4], p[5]), cvtpk(p[6], p[7])};
    const float a0 = dpp<0x00>(alpha), a1 = dpp<0x55>(alpha), a2 = dpp<0xAA>(alpha), a3 = dpp<0xFF>(alpha);
#pragma unroll
    for (int e = 0; e < 8; ++e) {
      acc[e][0] *= a0; acc[e][1] *= a1; acc[e][2] *= a2; acc[e][3] *= a3;
      v4u bf;
#pragma unroll
      for (int jj = 0; jj < 4; ++jj)
        bf[jj] = __builtin_amdgcn_perm(vr[2 * jj + 1][e >> 1], vr[2 * jj][e >> 1], (e & 1) ? 0x07060302u : 0x05040100u);
      acc[e] = mfma(pf, bf, acc[e]);
    }
  }
  l = xsum16_32(l);
  if (g == 0) {
#pragma unroll
    for (int r = 0; r < 4; ++r)
#pragma unroll
      for (int e = 0; e < 8; ++e) sm[w][r][4 + 8 * n + e] = acc[e][r];
    if (n < 4) { sm[w][n][0] = m; sm[w][n][1] = l; }
  }
  __syncthreads();
  const int r = tid >> 7, d = tid & 127;
  float M = -INFINITY;
#pragma unroll
  for (int i = 0; i < 8; ++i) M = fmaxf(M, sm[i][r][0]);
  float L = 0.f, o = 0.f;
#pragma unroll
  for (int i = 0; i < 8; ++i) { const float wt = __builtin_amdgcn_exp2f(sm[i][r][0] - M); L += wt * sm[i][r][1]; o += wt * sm[i][r][4 + d]; }
  float* rec = part + ((size_t)(kvh * 4 + r) * NSPLIT + sp) * (DH + 4);
  rec[4 + d] = o;
  if (d == 0) { rec[0] = M; rec[1] = L; }
}

__global__ void __launch_bounds__(128) k_comb(const float* part, float* o) {
  const int h = blockIdx.x, d = threadIdx.x;
  const float* rec = part + (size_t)h * NSPLIT * (DH + 4);
  float M = -INFINITY;
  for (int s = 0; s < NSPLIT; ++s) M = fmaxf(M, rec[s * (DH + 4)]);
  float L = 0.f, acc = 0.f;
  for (int s = 0; s < NSPLIT; ++s) { const float wt = __builtin_amdgcn_exp2f(rec[s * (DH + 4)] - M); L += wt * rec[s * (DH + 4) + 1]; acc += wt * rec[s * (DH + 4) + 4 + d]; }
  o[h * DH + d] = rbf(acc / L);
}

__global__ void __launch_bounds__(256) k_wo(const float* o, const u16* wo, const u16* x, const float* mod, float* x1) {
  __shared__ float a[HQ * DH]; __shared__ float red[32 * 64];
  for (int i = threadIdx.x; i < HQ * DH; i += 256) a[i] = o[i];
  __syncthreads();
  const float s = gemv64<HQ * DH, D>(a, wo, blockIdx.x * 64, red);
  if (threadIdx.x < 64) { const int n = blockIdx.x * 64 + threadIdx.x; x1[n] = rbf(bf2f(x[n]) + rbf(mod[2 * D + n] * rbf(s))); }
}

__global__ void __launch_bounds__(256) k_w1(const float* x1, const float* mod, const u16* w1, float* h1) {
  __shared__ float xs[D]; __shared__ float h[D]; __shared__ float red[32 * 64];
  for (int i = threadIdx.x; i < D; i += 256) xs[i] = x1[i];
  __syncthreads();
  ln_mod(xs, mod + 3 * D, mod + 4 * D, h, red);
  const float s = gemv64<D, FF>(h, w1, blockIdx.x * 64, red);
  if (threadIdx.x < 64) h1[blockIdx.x * 64 + threadIdx.x] = rbf(silu(rbf(s)));
}

__global__ void __launch_bounds__(256) k_w2(const float* h1, const u16* w2, const float* x1, const float* mod, u16* out) {
  __shared__ float a[FF]; __shared__ float red[32 * 64];
  for (int i = threadIdx.x; i < FF; i += 256) a[i] = h1[i];
  __syncthreads();
  const float s = gemv64<FF, D>(a, w2, blockIdx.x * 64, red);
  if (threadIdx.x < 64) { const int n = blockIdx.x * 64 + threadIdx.x; out[n] = (u16)(cvtpk(x1[n] + rbf(mod[5 * D + n] * rbf(s)), 0.f) & 0xffffu); }
}

extern "C" void kernel_launch(void* const* d_in, const int* in_sizes, int n_in, void* d_out, int out_size, void* d_ws, size_t ws_size,
                              hipStream_t stream) {
  const u16* x = (const u16*)d_in[0];
  const u16* kc = (const u16*)d_in[1];
  const u16* vc = (const u16*)d_in[2];
  const u16* wq = (const u16*)d_in[3];
  const u16* wo = (const u16*)d_in[4];
  const u16* w1 = (const u16*)d_in[6];
  const u16* w2 = (const u16*)d_in[7];
  const u16* c = (const u16*)d_in[8];
  const u16* wmod = (const u16*)d_in[9];
  char* ws = (char*)d_ws;
  float* mod = (float*)(ws);
  u16* q = (u16*)(ws + 16384);
  float* o = (float*)(ws + 32768);
  float* x1 = (float*)(ws + 65536);
  float* h1 = (float*)(ws + 69632);
  float* part = (float*)(ws + 131072);
  k_mod<<<48, 256, 0, stream>>>(c, wmod, mod);
  k_q<<<64, 256, 0, stream>>>(x, mod, wq, q);
  k_attn<<<256, 512, 0, stream>>>(kc, vc, q, part);
  k_comb<<<32, 128, 0, stream>>>(part, o);
  k_wo<<<8, 256, 0, stream>>>(o, wo, x, mod, x1);
  k_w1<<<28, 256, 0, stream>>>(x1, mod, w1, h1);
  k_w2<<<8, 256, 0, stream>>>(h1, w2, x1, mod, (u16*)d_out);
}
```

```cpp
#include <hip/hip_runtime.h>
#include <stdint.h>

#define GAS __attribute__((address_space(1)))
#define LAS __attribute__((address_space(3)))
typedef unsigned v4u __attribute__((ext_vector_type(4)));
typedef unsigned v2u __attribute__((ext_vector_type(2)));
typedef float f32x4 __attribute__((ext_vector_type(4)));
typedef short bf16x8 __attribute__((ext_vector_type(8)));
typedef __bf16 bf16x2 __attribute__((ext_vector_type(2)));
typedef unsigned short u16;
typedef unsigned long long u64;
typedef GAS unsigned gu32;
typedef GAS u64 gu64;
#define RLX_AGENT __ATOMIC_RELAXED, __HIP_MEMORY_SCOPE_AGENT

#define THR0 4

constexpr int D = 512, SK = 65536, DH = 128, FF = 1792;
constexpr int NWG = 256, NW = 8, NTHR = NW * 64;
constexpr int TPC = SK / 32 / 32;
constexpr int AUX_NT = 2, AUX_SC1 = 16;
constexpr unsigned SENT = 0xFFFFFFFFu;
constexpr unsigned SPIN_CAP = 1u << 16;
constexpr int EPOCH_OFF = 0;
constexpr int MODG_OFF = 4096;
constexpr int QG_OFF = 16384;
constexpr int MB_OFF = 65536;
constexpr int AO_OFF = MB_OFF;
constexpr int AML_OFF = AO_OFF + 512 * 1024;
constexpr int BY_OFF = AML_OFF + 8 * 1024;
constexpr int CO_OFF = BY_OFF + 32 * 1024;
constexpr int MB_BYTES = 64 * 11 * 1024;
constexpr int WS_BYTES = MB_OFF + MB_BYTES;
static_assert(CO_OFF + 56 * 2048 <= WS_BYTES, "mailboxes inside the cleared block");
constexpr int STAMP_OFF = 16 << 20;
constexpr int L_W = 0;
constexpr int L_MRG = 65536;
constexpr int L_Q = L_MRG + 8 * 4 * 132 * 4;
constexpr int L_ACT = L_Q + 1024;
constexpr int L_RED = L_ACT + 1024;
constexpr int L_ST = L_RED + 1024;
constexpr int L_H1 = L_ST + 64;
constexpr int L_X1 = L_H1 + 512;
constexpr int L_GB = L_X1 + 2048;
constexpr int L_CTL = L_GB + 2048;
constexpr int L_BYTES = L_CTL + 16;

__device__ __forceinline__ float lo16(unsigned w) { return __builtin_bit_cast(float, w << 16); }
__device__ __forceinline__ float hi16(unsigned w) { return __builtin_bit_cast(float, w & 0xffff0000u); }
__device__ __forceinline__ unsigned cvtpk(float lo, float hi) {
  bf16x2 r; r.x = (__bf16)lo; r.y = (__bf16)hi;
  return __builtin_bit_cast(unsigned, r);
}
__device__ __forceinline__ float rbf(float x) { return (float)(__bf16)x; }
__device__ __forceinline__ f32x4 mfma(v4u a, v4u b, f32x4 c) {
  return __builtin_amdgcn_mfma_f32_16x16x32_bf16(__builtin_bit_cast(bf16x8, a), __builtin_bit_cast(bf16x8, b), c, 0, 0, 0);
}
__device__ __forceinline__ float dot2(unsigned a, unsigned b, float acc) {
  return __builtin_amdgcn_fdot2_f32_bf16(__builtin_bit_cast(bf16x2, a), __builtin_bit_cast(bf16x2, b), acc, false);
}
__device__ __forceinline__ float silu(float x) { return x / (1.0f + __expf(-x)); }
template <int CTRL> __device__ __forceinline__ float dpp(float x) {
  return __builtin_bit_cast(float, __builtin_amdgcn_mov_dpp(__builtin_bit_cast(int, x), CTRL, 0xf, 0xf, true));
}
constexpr int XOR1 = 0xB1, XOR2 = 0x4E, ROR4 = 0x124, ROR8 = 0x128;
__device__ __forceinline__ float sum16(float x) {
  auto s = __builtin_amdgcn_permlane16_swap(__float_as_uint(x), __float_as_uint(x), false, false);
  return __uint_as_float(s[0]) + __uint_as_float(s[1]);
}
__device__ __forceinline__ float sum32(float x) {
  auto s = __builtin_amdgcn_permlane32_swap(__float_as_uint(x), __float_as_uint(x), false, false);
  return __uint_as_float(s[0]) + __uint_as_float(s[1]);
}
__device__ __forceinline__ float max16(float x) {
  auto s = __builtin_amdgcn_permlane16_swap(__float_as_uint(x), __float_as_uint(x), false, false);
  return fmaxf(__uint_as_float(s[0]), __uint_as_float(s[1]));
}
__device__ __forceinline__ float max32(float x) {
  auto s = __builtin_amdgcn_permlane32_swap(__float_as_uint(x), __float_as_uint(x), false, false);
  return fmaxf(__uint_as_float(s[0]), __uint_as_float(s[1]));
}
__device__ __forceinline__ float sum_b23(float x) { x += dpp<ROR4>(x); x += dpp<ROR8>(x); return x; }
__device__ __forceinline__ float wave_sum(float x) { x += dpp<XOR1>(x); x += dpp<XOR2>(x); x = sum_b23(x); return sum32(sum16(x)); }
#define LGKM0() asm volatile("s_waitcnt lgkmcnt(0)" ::: "memory")
#define VM0() asm volatile("s_waitcnt vmcnt(0)" ::: "memory")
#define BAR() do { LGKM0(); __builtin_amdgcn_s_barrier(); asm volatile("" ::: "memory"); } while (0)
#define SCHED() __builtin_amdgcn_sched_barrier(0)
__device__ __forceinline__ bool has_sent(v4u v) { return v.x == SENT || v.y == SENT || v.z == SENT || v.w == SENT; }
__device__ __forceinline__ __amdgpu_buffer_rsrc_t mkrs(const void* p, int bytes) { return __builtin_amdgcn_make_buffer_rsrc((void*)p, 0, bytes, 0x00020000); }
__device__ __forceinline__ v4u ldnt(__amdgpu_buffer_rsrc_t r, int vo, int so) { return __builtin_amdgcn_raw_buffer_load_b128(r, vo, so, AUX_NT); }
__device__ __forceinline__ v4u ldsc(__amdgpu_buffer_rsrc_t r, int vo, int so) { return __builtin_amdgcn_raw_buffer_load_b128(r, vo, so, AUX_SC1); }
__device__ __forceinline__ void stsc(v4u v, __amdgpu_buffer_rsrc_t r, int vo, int so) { __builtin_amdgcn_raw_buffer_store_b128(v, r, vo, so, AUX_SC1); }

#define STAMP(k) do {} while (0)

__device__ __forceinline__ void gemv16(const v4u (&W)[16], const LAS unsigned* act, int lane, float (&acc)[8]) {
  unsigned ap[8];
#pragma unroll
  for (int p = 0; p < 8; ++p) ap[p] = act[32 * p + (lane >> 1)];
#pragma unroll
  for (int e = 0; e < 8; ++e) acc[e] = 0.f;
#pragma unroll
  for (int p = 0; p < 8; ++p)
#pragma unroll
    for (int d = 0; d < 4; ++d) {
      acc[2 * d] = dot2(__builtin_amdgcn_perm(W[2 * p + 1][d], W[2 * p][d], 0x05040100u), ap[p], acc[2 * d]);
      acc[2 * d + 1] = dot2(__builtin_amdgcn_perm(W[2 * p + 1][d], W[2 * p][d], 0x07060302u), ap[p], acc[2 * d + 1]);
    }
#pragma unroll
  for (int e = 0; e < 8; ++e) { float x = acc[e]; x += dpp<XOR2>(x); x = sum_b23(x); acc[e] = sum32(sum16(x)); }
}
__device__ __forceinline__ void tr_store(const v4u* R, LAS unsigned char* la) {
#pragma unroll
  for (int e = 0; e < 8; ++e) {
    v4u u;
#pragma unroll
    for (int jj = 0; jj < 4; ++jj) u[jj] = __builtin_amdgcn_perm(R[2 * jj + 1][e >> 1], R[2 * jj][e >> 1], (e & 1) ? 0x07060302u : 0x05040100u);
    *(LAS v4u*)(la + e * 16) = u;
  }
}

__global__ void __launch_bounds__(NTHR, 2)
fused_decode(const u16* __restrict__ xin, const u16* __restrict__ kc, const u16* __restrict__ vc, const u16* __restrict__ wq,
             const u16* __restrict__ wo, const u16* __restrict__ w1, const u16* __restrict__ w2, const u16* __restrict__ cin,
             const u16* __restrict__ wmod, u16* __restrict__ out, unsigned char* ws) {
  extern __shared__ __attribute__((aligned(16))) unsigned char smem[];
  LAS unsigned char* L = (LAS unsigned char*)smem;
  volatile LAS unsigned* ctl = (volatile LAS unsigned*)(L + L_CTL);
  const int tid = threadIdx.x, lane = tid & 63, w = __builtin_amdgcn_readfirstlane(tid >> 6);
  const int g = lane >> 4, n = lane & 15;
  const int b = blockIdx.x, xg = b & 7, ig = b >> 3;
  const bool isC2 = ig >= 24, isC3 = (ig >= 16 && ig < 24 && xg < 7);
  const int hp = 2 * xg + ((ig - 24) >> 2), cq = ig & 3;
  const int j3 = 8 * xg + (ig - 16);
  const __amdgpu_buffer_rsrc_t rws = mkrs(ws, WS_BYTES);
  if (tid == 0) { ctl[0] = 0u; ctl[1] = 0u; ctl[3] = 0u; }
  BAR();
  unsigned tag = 0;
  if (w == 0) {
    __builtin_amdgcn_s_setprio(3);
    STAMP(0);
    tag = __hip_atomic_load((gu32*)(ws + EPOCH_OFF), RLX_AGENT) + 1u;
    const v4u c8 = *(const GAS v4u*)((const GAS u16*)cin + 8 * lane), x8 = *(const GAS v4u*)((const GAS u16*)xin + 8 * lane);
    v4u Wm[16], Wq[16];
    const int mcol = 64 * (xg + 8 * (ig >> 2)) + 16 * (ig & 3);
    {
      const __amdgpu_buffer_rsrc_t rm = mkrs(wmod, ig < 24 ? D * 6 * D * 2 : 0);
      const __amdgpu_buffer_rsrc_t rq = mkrs(wq, D * 4096 * 2);
      const int om = (lane >> 1) * (2 * 6 * D * 2) + (lane & 1) * 16, oq = (lane >> 1) * (2 * 4096 * 2) + (lane & 1) * 16;
#pragma unroll
      for (int u = 0; u < 16; ++u) Wm[u] = ldnt(rm, om, ((u >> 1) * 64 + (u & 1)) * (6 * D * 2) + mcol * 2);
#pragma unroll
      for (int u = 0; u < 16; ++u) Wq[u] = ldnt(rq, oq, ((u >> 1) * 64 + (u & 1)) * (4096 * 2) + (512 * xg + 16 * ig) * 2);
    }
    SCHED();
    LAS unsigned* act = (LAS unsigned*)(L + L_ACT);
    float nx[8];
    {
      v4u s;
#pragma unroll
      for (int d = 0; d < 4; ++d) s[d] = cvtpk(silu(lo16(c8[d])), silu(hi16(c8[d])));
      *(LAS v4u*)(act + 4 * lane) = s;
      float xv[8], sm = 0.f;
#pragma unroll
      for (int d = 0; d < 4; ++d) { xv[2 * d] = lo16(x8[d]); xv[2 * d + 1] = hi16(x8[d]); sm += xv[2 * d] + xv[2 * d + 1]; }
      const float mean = wave_sum(sm) * (1.0f / D);
      float sq = 0.f;
#pragma unroll
      for (int e = 0; e < 8; ++e) sq += (xv[e] - mean) * (xv[e] - mean);
      const float var = rbf(wave_sum(sq) * (1.0f / D));
      const float mb = rbf(mean), sd = rbf(sqrtf(rbf(var + 1e-5f)));
#pragma unroll
      for (int e = 0; e < 8; ++e) nx[e] = rbf(rbf(xv[e] - mb) / sd);
    }
    LGKM0();
    float acc[8];
    gemv16(Wm, act, lane, acc);
    if (ig < 24) {
      if (b < 64) { for (unsigned spins = 0; ctl[3] == 0u && spins < (1u << 22); ++spins) __builtin_amdgcn_s_sleep(1); }
      asm volatile("" ::: "memory");
      if (lane < 2) {
#pragma unroll
        for (int d = 0; d < 4; ++d)
          __hip_atomic_store((gu64*)(ws + MODG_OFF) + (mcol >> 1) + 4 * lane + d, ((u64)tag << 32) | cvtpk(acc[2 * d], acc[2 * d + 1]), RLX_AGENT);
      }
    }
    STAMP(1);
    {
      const gu64* gs = (const gu64*)(ws + MODG_OFF) + 4 * lane;
      u64 v[8];
      for (unsigned spins = 0;; ++spins) {
        bool ok = true;
#pragma unroll
        for (int d = 0; d < 8; ++d) v[d] = __hip_atomic_load((gu64*)gs + (d >> 2) * 256 + (d & 3), RLX_AGENT);
#pragma unroll
        for (int d = 0; d < 8; ++d) ok &= (unsigned)(v[d] >> 32) == tag;
        if (__all(ok) || spins > SPIN_CAP) break;
      }
      v4u h;
#pragma unroll
      for (int d = 0; d < 4; ++d) {
        const unsigned sa = (unsigned)v[d], ca = (unsigned)v[4 + d];
        h[d] = cvtpk(rbf(nx[2 * d] * rbf(1.0f + lo16(ca))) + lo16(sa), rbf(nx[2 * d + 1] * rbf(1.0f + hi16(ca))) + hi16(sa));
      }
      *(LAS v4u*)(act + 4 * lane) = h;
      LGKM0();
    }
    STAMP(2);
    gemv16(Wq, act, lane, acc);
    if (lane < 2) {
#pragma unroll
      for (int d = 0; d < 4; ++d)
        __hip_atomic_store((gu64*)(ws + QG_OFF) + 256 * xg + 8 * ig + 4 * lane + d, ((u64)tag << 32) | cvtpk(acc[2 * d], acc[2 * d + 1]), RLX_AGENT);
    }
    STAMP(3);
    {
      const gu64* gs = (const gu64*)(ws + QG_OFF) + 256 * xg + lane;
      u64 v[4];
      for (unsigned spins = 0;; ++spins) {
        bool ok = true;
#pragma unroll
        for (int d = 0; d < 4; ++d) v[d] = __hip_atomic_load((gu64*)gs + 64 * d, RLX_AGENT);
#pragma unroll
        for (int d = 0; d < 4; ++d) ok &= (unsigned)(v[d] >> 32) == tag;
        if (__all(ok) || spins > SPIN_CAP) break;
      }
#pragma unroll
      for (int d = 0; d < 4; ++d) ((LAS unsigned*)(L + L_Q))[64 * d + lane] = (unsigned)v[d];
      ctl[2] = tag;
      LGKM0();
      ctl[1] = 1u;
    }
    STAMP(4);
    __builtin_amdgcn_s_setprio(0);
    if (isC2) {
      const __amdgpu_buffer_rsrc_t r = mkrs(wo, 4096 * D * 2);
      const int vo = (8 * g) * (D * 2) + n * 16;
#pragma unroll 1
      for (int hb = 0; hb < 2; ++hb) {
        v4u R[32];
#pragma unroll
        for (int u = 0; u < 32; ++u) R[u] = ldnt(r, vo, (256 * hp + 128 * hb + 32 * (u >> 3) + (u & 7)) * (D * 2) + cq * 256);
        SCHED();
#pragma unroll
        for (int k = 0; k < 4; ++k) tr_store(&R[8 * k], L + L_W + ((((n >> 1) * 8 + 4 * hb + k) * 64) + g * 16 + 8 * (n & 1)) * 16);
      }
    } else if (isC3) {
      {
        const __amdgpu_buffer_rsrc_t r = mkrs(w1, D * FF * 2);
        const int ro = lane >> 2, c4 = lane & 3;
        const int vo = (8 * ro) * (FF * 2) + c4 * 16;
        v4u R[32];
#pragma unroll
        for (int u = 0; u < 32; ++u) R[u] = ldnt(r, vo, (128 * (u >> 3) + (u & 7)) * (FF * 2) + j3 * 64);
        SCHED();
#pragma unroll
        for (int k = 0; k < 4; ++k) tr_store(&R[8 * k], L + L_W + ((((4 * k + (ro >> 2)) * 2 + (c4 >> 1)) * 64) + (ro & 3) * 16 + 8 * (c4 & 1)) * 16);
      }
      {
        const __amdgpu_buffer_rsrc_t r = mkrs(w2, FF * D * 2);
        const int vo = (8 * g) * (D * 2) + n * 16;
        v4u R[32];
#pragma unroll
        for (int u = 0; u < 32; ++u) R[u] = ldnt(r, vo, (32 * j3 + (u & 7)) * (D * 2) + (u >> 3) * 256);
        SCHED();
#pragma unroll
        for (int k = 0; k < 4; ++k) tr_store(&R[8 * k], L + L_W + 32768 + (((8 * k + (n >> 1)) * 64) + g * 16 + 8 * (n & 1)) * 16);
      }
    }
    STAMP(5);
  } else if (w == 7 && b < 64) {
    const v4u s4 = {SENT, SENT, SENT, SENT};
#pragma unroll
    for (int i = 0; i < 11; ++i) stsc(s4, rws, lane * 16, MB_OFF + b * 11264 + i * 1024);
    VM0();
    ctl[3] = 1u;
  }
  {
    constexpr float C2 = 0.088388347648318440f * 1.4426950408889634f;
    const __amdgpu_buffer_rsrc_t rk = mkrs(kc + ((size_t)xg * SK + (size_t)ig * 2048) * DH, TPC * 8192);
    const __amdgpu_buffer_rsrc_t rv = mkrs(vc + ((size_t)xg * SK + (size_t)ig * 2048) * DH, TPC * 8192);
    const int koff = (4 * (n & 3) + (n >> 2)) * 256 + 16 * g, voff = lane * 16;
    v4u kf[2][8], vr[2][8];
    int id[2];
    auto claim = [&](unsigned k) -> int {
      unsigned c = 0;
      if (lane == 0) c = __hip_atomic_fetch_add((LAS unsigned*)(L + L_CTL), k, __ATOMIC_RELAXED, __HIP_MEMORY_SCOPE_WORKGROUP);
      return __builtin_amdgcn_readfirstlane(c);
    };
#define THROTTLE() asm volatile("s_waitcnt vmcnt(%0)" ::"i"(THR0 - 1) : "memory")
#define LOADK(d, T) { const int so_ = id[d] * 8192; _Pragma("unroll") for (int u = 0; u < 8; ++u) { T; kf[d][u] = ldnt(rk, koff, so_ + (u >> 2) * 4096 + (u & 3) * 64); } }
#define LOADV(d, T) { const int so_ = id[d] * 8192; _Pragma("unroll") for (int u = 0; u < 8; ++u) { T; vr[d][u] = ldnt(rv, voff, so_ + u * 1024); } }
    id[0] = claim(2); id[1] = id[0] + 1;
    LOADK(0, THROTTLE()); LOADV(0, THROTTLE()); LOADK(1, THROTTLE()); LOADV(1, THROTTLE());
    SCHED();
    for (unsigned spins = 0; ctl[1] == 0u && spins < (1u << 22); ++spins) __builtin_amdgcn_s_sleep(2);
    asm volatile("" ::: "memory");
    v4u qf[4];
#pragma unroll
    for (int s = 0; s < 4; ++s) qf[s] = *(const LAS v4u*)(L + L_Q + (n & 3) * 256 + 64 * s + 16 * g);
    f32x4 acc[8];
#pragma unroll
    for (int e = 0; e < 8; ++e) acc[e] = f32x4{0.f, 0.f, 0.f, 0.f};
    float m = -1e30f, l = 0.f;
    if (w == 1) STAMP(6);
#define STEP(d) { \
      const bool valid = id[d] < TPC; \
      const int nid = claim(1); \
      f32x4 sc[2]; \
      _Pragma("unroll") for (int t = 0; t < 2; ++t) { \
        sc[t] = f32x4{0.f, 0.f, 0.f, 0.f}; \
        _Pragma("unroll") for (int s = 0; s < 4; ++s) sc[t] = mfma(kf[d][t * 4 + s], qf[s], sc[t]); } \
      SCHED(); \
      id[d] = nid; \
      LOADK(d, (void)0); \
      SCHED(); \
      float tm = fmaxf(fmaxf(fmaxf(sc[0][0], sc[0][1]), fmaxf(sc[0][2], sc[0][3])), fmaxf(fmaxf(sc[1][0], sc[1][1]), fmaxf(sc[1][2], sc[1][3]))); \
      tm = max32(max16(tm)); \
      const float mn = valid ? fmaxf(m, tm * C2) : m; \
      const float alpha = __builtin_amdgcn_exp2f(m - mn); \
      m = mn; \
      const float msub = valid ? mn : INFINITY; \
      float p[8]; \
      _Pragma("unroll") for (int j = 0; j < 8; ++j) p[j] = __builtin_amdgcn_exp2f(sc[j >> 2][j & 3] * C2 - msub); \
      l = l * alpha + (((p[0] + p[1]) + (p[2] + p[3])) + ((p[4] + p[5]) + (p[6] + p[7]))); \
      const v4u pf = {cvtpk(p[0], p[1]), cvtpk(p[2], p[3]), cvtpk(p[4], p[5]), cvtpk(p[6], p[7])}; \
      const float a0 = dpp<0x00>(alpha), a1 = dpp<0x55>(alpha), a2 = dpp<0xAA>(alpha), a3 = dpp<0xFF>(alpha); \
      _Pragma("unroll") for (int e = 0; e < 8; ++e) { \
        acc[e][0] *= a0; acc[e][1] *= a1; acc[e][2] *= a2; acc[e][3] *= a3; \
        v4u bf; \
        _Pragma("unroll") for (int jj = 0; jj < 4; ++jj) \
          bf[jj] = __builtin_amdgcn_perm(vr[d][2 * jj + 1][e >> 1], vr[d][2 * jj][e >> 1], (e & 1) ? 0x07060302u : 0x05040100u); \
        acc[e] = mfma(pf, bf, acc[e]); } \
      SCHED(); \
      LOADV(d, (void)0); \
      SCHED(); }
#pragma unroll 1
    do { STEP(0) STEP(1) } while (id[0] < TPC);
    l = sum32(sum16(l));
    LAS float* mrg = (LAS float*)(L + L_MRG);
    if (g == 0) {
#pragma unroll
      for (int r = 0; r < 4; ++r) {
        *(LAS f32x4*)(mrg + (w * 4 + r) * 132 + 4 + 8 * n) = f32x4{acc[0][r], acc[1][r], acc[2][r], acc[3][r]};
        *(LAS f32x4*)(mrg + (w * 4 + r) * 132 + 8 + 8 * n) = f32x4{acc[4][r], acc[5][r], acc[6][r], acc[7][r]};
      }
      if (n < 4) { mrg[(w * 4 + n) * 132] = m; mrg[(w * 4 + n) * 132 + 1] = l; }
    }
    BAR();
    if (w == 0) STAMP(7);
    if (w < 2) {
      const int r = tid >> 5, dc = tid & 31;
      float M = -1e30f;
#pragma unroll
      for (int i = 0; i < NW; ++i) M = fmaxf(M, mrg[(i * 4 + r) * 132]);
      float Ls = 0.f; f32x4 o = {0.f, 0.f, 0.f, 0.f};
#pragma unroll
      for (int i = 0; i < NW; ++i) {
        const float wt = __builtin_amdgcn_exp2f(mrg[(i * 4 + r) * 132] - M);
        Ls += wt * mrg[(i * 4 + r) * 132 + 1];
        o += wt * *(const LAS f32x4*)(mrg + (i * 4 + r) * 132 + 4 + 4 * dc);
      }
      stsc(__builtin_bit_cast(v4u, o), rws, lane * 16, AO_OFF + ((2 * xg + w) * 32 + ig) * 1024);
      if (dc == 0)
        __hip_atomic_store((gu64*)(ws + AML_OFF) + ((2 * xg + w) * 32 + ig) * 2 + (r & 1), ((u64)__float_as_uint(Ls) << 32) | __float_as_uint(M), RLX_AGENT);
    }
    if (w == 0) STAMP(8);
  }
  if (isC2) {
    v4u wf[8];
#pragma unroll
    for (int k = 0; k < 8; ++k) wf[k] = *(const LAS v4u*)(L + L_W + ((w * 8 + k) * 64 + lane) * 16);
    const int sg = lane >> 3, dc = lane & 7, hh = w >> 2;
    v4u o4[4]; u64 ml;
    for (unsigned spins = 0;; ++spins) {
      asm volatile("" ::: "memory");
#pragma unroll
      for (int i = 0; i < 4; ++i) o4[i] = ldsc(rws, sg * 1024 + dc * 16, AO_OFF + hp * 32768 + i * 8192 + w * 128);
      ml = __hip_atomic_load((gu64*)(ws + AML_OFF) + hp * 64 + lane, RLX_AGENT);
      const bool bad = has_sent(o4[0]) || has_sent(o4[1]) || has_sent(o4[2]) || has_sent(o4[3]) || (unsigned)ml == SENT || (unsigned)(ml >> 32) == SENT;
      if (!__any(bad) || spins > SPIN_CAP) break;
    }
    if (w == 0) STAMP(9);
    const float ms = __uint_as_float((unsigned)ml), ls = __uint_as_float((unsigned)(ml >> 32));
    float M = fmaxf(ms, dpp<XOR2>(ms)); M = fmaxf(M, dpp<ROR4>(M)); M = fmaxf(M, dpp<ROR8>(M)); M = max32(max16(M));
    const float wt = __builtin_amdgcn_exp2f(ms - M);
    float Lt = wt * ls; Lt += dpp<XOR2>(Lt); Lt = sum_b23(Lt); Lt = sum32(sum16(Lt));
    const float wn = wt / Lt;
    f32x4 o = {0.f, 0.f, 0.f, 0.f};
#pragma unroll
    for (int i = 0; i < 4; ++i) {
      const float wi = __builtin_bit_cast(float, __builtin_amdgcn_ds_bpermute(((sg + 8 * i) * 2 + hh) * 4, __builtin_bit_cast(int, wn)));
      o += wi * __builtin_bit_cast(f32x4, o4[i]);
    }
#pragma unroll
    for (int e = 0; e < 4; ++e) { float x = o[e]; x += dpp<ROR8>(x); o[e] = sum32(sum16(x)); }
    if (lane < 8) *(LAS v2u*)(L + L_ACT + w * 64 + dc * 8) = v2u{cvtpk(o[0], o[1]), cvtpk(o[2], o[3])};
    BAR();
    f32x4 y0 = {0.f, 0.f, 0.f, 0.f}, y1 = {0.f, 0.f, 0.f, 0.f};
#pragma unroll
    for (int k = 0; k < 8; k += 2) {
      y0 = mfma(wf[k], *(const LAS v4u*)(L + L_ACT + k * 64 + g * 16), y0);
      y1 = mfma(wf[k + 1], *(const LAS v4u*)(L + L_ACT + (k + 1) * 64 + g * 16), y1);
    }
    y0 += y1;
    if (n == 0) stsc(__builtin_bit_cast(v4u, y0), rws, g * 16, BY_OFF + hp * 2048 + cq * 512 + w * 64);
    if (w == 0) STAMP(10);
  }
  if (isC3) {
    tag = ctl[2];
    v4u f1[4], f2[4];
#pragma unroll
    for (int k = 0; k < 4; ++k) f1[k] = *(const LAS v4u*)(L + L_W + ((w * 4 + k) * 64 + lane) * 16);
#pragma unroll
    for (int k = 0; k < 4; ++k) f2[k] = *(const LAS v4u*)(L + L_W + 32768 + ((w * 4 + k) * 64 + lane) * 16);
    const int col = 64 * w + 4 * n;
    float ga[4], sb[4], cb[4], gb[4], xv[4];
    {
      const gu64* gs = (const gu64*)(ws + MODG_OFF) + (col >> 1);
      u64 v[8];
      for (unsigned spins = 0;; ++spins) {
        bool ok = true;
#pragma unroll
        for (int d = 0; d < 8; ++d) v[d] = __hip_atomic_load((gu64*)gs + (2 + (d >> 1)) * 256 + (d & 1), RLX_AGENT);
#pragma unroll
        for (int d = 0; d < 8; ++d) ok &= (unsigned)(v[d] >> 32) == tag;
        if (__all(ok) || spins > SPIN_CAP) break;
      }
#pragma unroll
      for (int d = 0; d < 2; ++d) {
        ga[2 * d] = lo16((unsigned)v[d]); ga[2 * d + 1] = hi16((unsigned)v[d]);
        sb[2 * d] = lo16((unsigned)v[2 + d]); sb[2 * d + 1] = hi16((unsigned)v[2 + d]);
        cb[2 * d] = rbf(1.0f + lo16((unsigned)v[4 + d])); cb[2 * d + 1] = rbf(1.0f + hi16((unsigned)v[4 + d]));
        gb[2 * d] = lo16((unsigned)v[6 + d]); gb[2 * d + 1] = hi16((unsigned)v[6 + d]);
      }
      const v2u x2 = *(const GAS v2u*)((const GAS u16*)xin + col);
      xv[0] = lo16(x2.x); xv[1] = hi16(x2.x); xv[2] = lo16(x2.y); xv[3] = hi16(x2.y);
    }
    v4u yp[4];
    for (unsigned spins = 0;; ++spins) {
      asm volatile("" ::: "memory");
#pragma unroll
      for (int i = 0; i < 4; ++i) yp[i] = ldsc(rws, g * 2048 + n * 16, BY_OFF + i * 8192 + w * 256);
      const bool bad = has_sent(yp[0]) || has_sent(yp[1]) || has_sent(yp[2]) || has_sent(yp[3]);
      if (!__any(bad) || spins > SPIN_CAP) break;
    }
    if (w == 0) STAMP(11);
    const f32x4 ys = (__builtin_bit_cast(f32x4, yp[0]) + __builtin_bit_cast(f32x4, yp[1])) + (__builtin_bit_cast(f32x4, yp[2]) + __builtin_bit_cast(f32x4, yp[3]));
    float x1[4], s1 = 0.f, s2 = 0.f;
#pragma unroll
    for (int e = 0; e < 4; ++e) { x1[e] = rbf(xv[e] + rbf(ga[e] * rbf(sum32(sum16(ys[e]))))); s1 += x1[e]; s2 += x1[e] * x1[e]; }
    s1 += dpp<XOR1>(s1); s1 += dpp<XOR2>(s1); s1 = sum_b23(s1);
    s2 += dpp<XOR1>(s2); s2 += dpp<XOR2>(s2); s2 = sum_b23(s2);
    if (lane == 0) *(LAS v2u*)(L + L_ST + w * 8) = v2u{__float_as_uint(s1), __float_as_uint(s2)};
    if (g == 0) {
      *(LAS f32x4*)(L + L_X1 + col * 4) = f32x4{x1[0], x1[1], x1[2], x1[3]};
      *(LAS f32x4*)(L + L_GB + col * 4) = f32x4{gb[0], gb[1], gb[2], gb[3]};
    }
    BAR();
    {
      float t1 = 0.f, t2 = 0.f;
#pragma unroll
      for (int i = 0; i < 4; ++i) { const f32x4 t = *(const LAS f32x4*)(L + L_ST + i * 16); t1 += t[0] + t[2]; t2 += t[1] + t[3]; }
      const float mean = t1 * (1.0f / D);
      const float var = rbf(fmaxf(t2 * (1.0f / D) - mean * mean, 0.f));
      const float mb = rbf(mean), sd = rbf(sqrtf(rbf(var + 1e-5f)));
      float h[4];
#pragma unroll
      for (int e = 0; e < 4; ++e) h[e] = rbf(rbf(rbf(x1[e] - mb) / sd) * cb[e]) + sb[e];
      if (g == 0) *(LAS v2u*)(L + L_ACT + col * 2) = v2u{cvtpk(h[0], h[1]), cvtpk(h[2], h[3])};
      LGKM0();
    }
    {
      const v4u a0 = *(const LAS v4u*)(L + L_ACT + w * 128 + g * 16), a1 = *(const LAS v4u*)(L + L_ACT + w * 128 + 64 + g * 16);
      const f32x4 z = {0.f, 0.f, 0.f, 0.f};
      const f32x4 t0 = mfma(f1[2], a1, mfma(f1[0], a0, z)), t1 = mfma(f1[3], a1, mfma(f1[1], a0, z));
      if (n < 2) *(LAS f32x4*)(L + L_RED + w * 128 + n * 64 + g * 16) = n == 0 ? t0 : t1;
    }
    BAR();
    {
      float s = 0.f;
#pragma unroll
      for (int i = 0; i < NW; ++i) s += ((const LAS float*)(L + L_RED))[i * 32 + (lane & 31)];
      const float hv = silu(rbf(s));
      if (lane < 32) ((LAS u16*)(L + L_H1))[w * 32 + lane] = (u16)cvtpk(hv, 0.f);
      LGKM0();
      const v4u a = *(const LAS v4u*)(L + L_H1 + w * 64 + g * 16);
      const f32x4 z = {0.f, 0.f, 0.f, 0.f};
      const f32x4 t0 = mfma(f2[0], a, z), t1 = mfma(f2[1], a, z), t2 = mfma(f2[2], a, z), t3 = mfma(f2[3], a, z);
      const f32x4 t = n == 0 ? t0 : n == 1 ? t1 : n == 2 ? t2 : t3;
      if (n < 4) stsc(__builtin_bit_cast(v4u, t), rws, n * 64 + g * 16, CO_OFF + j3 * 2048 + w * 256);
    }
    if (w == 0) STAMP(12);
    if (j3 < 32 && w == 0) {
      const int p = lane >> 2, cc = lane & 3;
      const __amdgpu_buffer_rsrc_t rc = mkrs(ws + CO_OFF, 56 * 2048);
      v4u cp[4];
      for (unsigned spins = 0;; ++spins) {
        asm volatile("" ::: "memory");
#pragma unroll
        for (int i = 0; i < 4; ++i) cp[i] = ldsc(rc, p * 2048 + cc * 16, i * 32768 + j3 * 64);
        const bool bad = has_sent(cp[0]) || has_sent(cp[1]) || has_sent(cp[2]) || has_sent(cp[3]);
        if (!__any(bad) || spins > SPIN_CAP) break;
      }
      STAMP(13);
      const f32x4 os = (__builtin_bit_cast(f32x4, cp[0]) + __builtin_bit_cast(f32x4, cp[1])) + (__builtin_bit_cast(f32x4, cp[2]) + __builtin_bit_cast(f32x4, cp[3]));
      const f32x4 xr = *(const LAS f32x4*)(L + L_X1 + (16 * j3 + 4 * cc) * 4), gr = *(const LAS f32x4*)(L + L_GB + (16 * j3 + 4 * cc) * 4);
      float o[4];
#pragma unroll
      for (int e = 0; e < 4; ++e) o[e] = xr[e] + rbf(gr[e] * rbf(sum32(sum16(sum_b23(os[e])))));
      if (lane < 4) *(GAS v2u*)((GAS u16*)out + 16 * j3 + 4 * cc) = v2u{cvtpk(o[0], o[1]), cvtpk(o[2], o[3])};
      if (j3 == 0 && lane == 0) __hip_atomic_store((gu32*)(ws + EPOCH_OFF), tag, RLX_AGENT);
      STAMP(14);
    }
  }
}

extern "C" void kernel_launch(void* const* d_in, const int* in_sizes, int n_in, void* d_out, int out_size, void* d_ws, size_t ws_size,
                              hipStream_t stream) {
  (void)in_sizes; (void)n_in; (void)out_size; (void)ws_size;
  (void)hipFuncSetAttribute((const void*)fused_decode, hipFuncAttributeMaxDynamicSharedMemorySize, L_BYTES);
  fused_decode<<<NWG, NTHR, L_BYTES, stream>>>((const u16*)d_in[0], (const u16*)d_in[1], (const u16*)d_in[2], (const u16*)d_in[3], (const u16*)d_in[4],
                                               (const u16*)d_in[6], (const u16*)d_in[7], (const u16*)d_in[8], (const u16*)d_in[9], (u16*)d_out,
                                               (unsigned char*)d_ws);
}
```

```cpp
#include <hip/hip_runtime.h>
#include <stdint.h>

#define GAS __attribute__((address_space(1)))
#define LAS __attribute__((address_space(3)))
typedef unsigned v4u __attribute__((ext_vector_type(4)));
typedef unsigned v2u __attribute__((ext_vector_type(2)));
typedef float f32x4 __attribute__((ext_vector_type(4)));
typedef float f32x2 __attribute__((ext_vector_type(2)));
typedef short bf16x8 __attribute__((ext_vector_type(8)));
typedef __bf16 bf16x2 __attribute__((ext_vector_type(2)));
typedef unsigned short u16;
typedef unsigned long long u64;
typedef GAS unsigned gu32;
typedef GAS u64 gu64;
#define RLX_AGENT __ATOMIC_RELAXED, __HIP_MEMORY_SCOPE_AGENT

#define DIST0 2
#define DIST 2
#define DISTE 7
#define KEND 6
#define SIGK 2
#define SLP1 4
#define SLP2 8
#define DRY 1
#define T_C2 62
#define T_A 65
#define T_B 64
static_assert(16 * T_A + 8 * T_B + 8 * T_C2 == 2048, "tiles of one kv head");

constexpr int D = 512, SK = 65536, DH = 128, FF = 1792;
constexpr int NWG = 256, NW = 8, NTHR = NW * 64;
constexpr int AUX_NT = 2, AUX_SC1 = 16;
constexpr unsigned SENT = 0xFFFFFFFFu;
constexpr unsigned SPIN_CAP = 1u << 16;
constexpr int OOB = 0x40000000;
constexpr int EPOCH_OFF = 0;
constexpr int MODG_OFF = 4096;
constexpr int QG_OFF = 16384;
constexpr int AREC = 1152;
constexpr int AGRP = 16 * 5120;
constexpr int AO_OFF = 65536;
constexpr int BY_OFF = AO_OFF + 8 * AGRP;
constexpr int CO_OFF = BY_OFF + 32 * 1024;
constexpr int WS_BYTES = BY_OFF + 32 * 5120;
static_assert(64 * AREC <= AGRP && CO_OFF + 56 * 2048 <= WS_BYTES, "mailboxes inside the cleared blocks");
constexpr int STAMP_OFF = 16 << 20;
constexpr int L_W = 0;
constexpr int L_MO = 65536;
constexpr int L_MM = L_MO + 16384;
constexpr int L_ML = L_MM + 128;
constexpr int L_Q = L_ML + 128;
constexpr int L_ACT = L_Q + 1024;
constexpr int L_RED = L_ACT + 1024;
constexpr int L_ST = L_RED + 1024;
constexpr int L_H1 = L_ST + 64;
constexpr int L_X1 = L_H1 + 512;
constexpr int L_MOD = L_X1 + 2048;
constexpr int L_CTL = L_MOD + 5120;
constexpr int L_STAMP = L_CTL + 16;
constexpr int L_BYTES = L_STAMP + 1024;

__device__ __forceinline__ float lo16(unsigned w) { return __builtin_bit_cast(float, w << 16); }
__device__ __forceinline__ float hi16(unsigned w) { return __builtin_bit_cast(float, w & 0xffff0000u); }
__device__ __forceinline__ unsigned cvtpk(float lo, float hi) {
  bf16x2 r; r.x = (__bf16)lo; r.y = (__bf16)hi;
  return __builtin_bit_cast(unsigned, r);
}
#define rbf(x) __builtin_bit_cast(float, cvtpk(zero_, (x)))
__device__ __forceinline__ f32x4 mfma(v4u a, v4u b, f32x4 c) {
  return __builtin_amdgcn_mfma_f32_16x16x32_bf16(__builtin_bit_cast(bf16x8, a), __builtin_bit_cast(bf16x8, b), c, 0, 0, 0);
}
__device__ __forceinline__ float dot2(unsigned a, unsigned b, float acc) {
  return __builtin_amdgcn_fdot2_f32_bf16(__builtin_bit_cast(bf16x2, a), __builtin_bit_cast(bf16x2, b), acc, false);
}
__device__ __forceinline__ float rcp(float x) { return __builtin_amdgcn_rcpf(x); }
__device__ __forceinline__ float ex2(float x) { return __builtin_amdgcn_exp2f(x); }
__device__ __forceinline__ float silu(float x) { return x * rcp(1.0f + ex2(x * -1.4426950408889634f)); }
template <int CTRL> __device__ __forceinline__ float dpp(float x) {
  return __builtin_bit_cast(float, __builtin_amdgcn_mov_dpp(__builtin_bit_cast(int, x), CTRL, 0xf, 0xf, true));
}
constexpr int XOR1 = 0xB1, XOR2 = 0x4E, HMIR = 0x141, ROR4 = 0x124, ROR8 = 0x128;
__device__ __forceinline__ float sum16(float x) {
  auto s = __builtin_amdgcn_permlane16_swap(__float_as_uint(x), __float_as_uint(x), false, false);
  return __uint_as_float(s[0]) + __uint_as_float(s[1]);
}
__device__ __forceinline__ float sum32(float x) {
  auto s = __builtin_amdgcn_permlane32_swap(__float_as_uint(x), __float_as_uint(x), false, false);
  return __uint_as_float(s[0]) + __uint_as_float(s[1]);
}
__device__ __forceinline__ float max16(float x) {
  auto s = __builtin_amdgcn_permlane16_swap(__float_as_uint(x), __float_as_uint(x), false, false);
  return fmaxf(__uint_as_float(s[0]), __uint_as_float(s[1]));
}
__device__ __forceinline__ float max32(float x) {
  auto s = __builtin_amdgcn_permlane32_swap(__float_as_uint(x), __float_as_uint(x), false, false);
  return fmaxf(__uint_as_float(s[0]), __uint_as_float(s[1]));
}
__device__ __forceinline__ float sum_q(float x) { x += dpp<XOR1>(x); x += dpp<XOR2>(x); return x; }
__device__ __forceinline__ float sum_8(float x) { x = sum_q(x); x += dpp<HMIR>(x); return x; }
__device__ __forceinline__ float sum_b23(float x) { x += dpp<ROR4>(x); x += dpp<ROR8>(x); return x; }
__device__ __forceinline__ float sum_row(float x) { return sum_b23(sum_q(x)); }
__device__ __forceinline__ float wave_sum(float x) { return sum32(sum16(sum_row(x))); }
__device__ __forceinline__ float quad_tr(f32x4 v, int lane) {
  const bool b0 = lane & 1, b1 = lane & 2;
  const float t0 = (b0 ? v[1] : v[0]) + dpp<XOR1>(b0 ? v[0] : v[1]);
  const float t1 = (b0 ? v[3] : v[2]) + dpp<XOR1>(b0 ? v[2] : v[3]);
  return (b1 ? t1 : t0) + dpp<XOR2>(b1 ? t0 : t1);
}
__device__ __forceinline__ float tr16(float a, float b) {
  auto s = __builtin_amdgcn_permlane16_swap(__float_as_uint(a), __float_as_uint(b), false, false);
  return __uint_as_float(s[0]) + __uint_as_float(s[1]);
}
__device__ __forceinline__ float tr32(float a, float b) {
  auto s = __builtin_amdgcn_permlane32_swap(__float_as_uint(a), __float_as_uint(b), false, false);
  return __uint_as_float(s[0]) + __uint_as_float(s[1]);
}
__device__ __forceinline__ float row_tr(float a, float b, float c, float d) { return tr32(tr16(a, b), tr16(c, d)); }
#define LGKM0() asm volatile("s_waitcnt lgkmcnt(0)" ::: "memory")
#define VM0() asm volatile("s_waitcnt vmcnt(0)" ::: "memory")
#define BAR() do { LGKM0(); __builtin_amdgcn_s_barrier(); asm volatile("" ::: "memory"); } while (0)
#define SCHED() __builtin_amdgcn_sched_barrier(0)
#define CBAR() asm volatile("" ::: "memory")
__device__ __forceinline__ unsigned umax(unsigned a, unsigned b) { return a > b ? a : b; }
__device__ __forceinline__ unsigned umax4(v4u v) { return umax(umax(v.x, v.y), umax(v.z, v.w)); }
__device__ __forceinline__ __amdgpu_buffer_rsrc_t mkrs(const void* p, int bytes) { return __builtin_amdgcn_make_buffer_rsrc((void*)p, 0, bytes, 0x00020000); }
__device__ __forceinline__ v4u ldnt(__amdgpu_buffer_rsrc_t r, int vo, int so) { return __builtin_amdgcn_raw_buffer_load_b128(r, vo, so, AUX_NT); }
__device__ __forceinline__ v4u ldsc(__amdgpu_buffer_rsrc_t r, int vo, int so) { return __builtin_amdgcn_raw_buffer_load_b128(r, vo, so, AUX_SC1); }
__device__ __forceinline__ void stsc(v4u v, __amdgpu_buffer_rsrc_t r, int vo, int so) { __builtin_amdgcn_raw_buffer_store_b128(v, r, vo, so, AUX_SC1); }
__device__ __forceinline__ f32x4 asf(v4u v) { return __builtin_bit_cast(f32x4, v); }
__device__ __forceinline__ v4u asu(f32x4 v) { return __builtin_bit_cast(v4u, v); }
#define PLO 0x05040100u
#define PHI 0x07060302u

#define STAMP(k) do {} while (0)

__global__ void __launch_bounds__(NTHR, 2)
fused_decode(const u16* __restrict__ xin, const u16* __restrict__ kc, const u16* __restrict__ vc, const u16* __restrict__ wq,
             const u16* __restrict__ wo, const u16* __restrict__ w1, const u16* __restrict__ w2, const u16* __restrict__ cin,
             const u16* __restrict__ wmod, u16* __restrict__ out, unsigned char* ws) {
  extern __shared__ __attribute__((aligned(16))) unsigned char smem[];
  LAS unsigned char* L = (LAS unsigned char*)smem;
  volatile LAS unsigned* ctl = (volatile LAS unsigned*)(L + L_CTL);
  const int w_0 = __builtin_amdgcn_readfirstlane(threadIdx.x >> 6);
#define INDICES() \
  int lane = threadIdx.x & 63, w = w_0, b = blockIdx.x; \
  asm volatile("" : "+v"(lane)); asm volatile("" : "+s"(w)); asm volatile("" : "+s"(b)); \
  float zero_ = 0.f; asm volatile("" : "+v"(zero_)); \
  const int g = lane >> 4, n = lane & 15, xg = b & 7, ig = b >> 3; \
  const bool isC2 = ig >= 24, isC3 = ig >= 16 && ig < 24 && xg < 7; \
  const int hp = 2 * xg + ((ig - 24) >> 2), cq = ig & 3;        \
  const int j3 = 8 * xg + (ig - 16);                            \
  (void)g; (void)n; (void)isC2; (void)isC3; (void)hp; (void)cq; (void)j3;
  asm volatile("" ::"s"(xin), "s"(kc), "s"(vc), "s"(wq), "s"(wo), "s"(w1), "s"(w2), "s"(cin), "s"(wmod), "s"(out), "s"(ws));
  const __amdgpu_buffer_rsrc_t rws = mkrs(ws, WS_BYTES);
  if (threadIdx.x == 0) { ctl[0] = 0u; ctl[2] = 0u; }
  BAR();
  LAS float* red = (LAS float*)(L + L_RED);
  if (w_0 == 0) {
    INDICES();
    __builtin_amdgcn_s_setprio(3);
    STAMP(1);
    const unsigned tag = __hip_atomic_load((gu32*)(ws + EPOCH_OFF), RLX_AGENT) + 1u;
    if (ig < 28) {
      const bool isq = ig >= 4 && ig < 20;
      const int c0 = ig < 4 ? 128 * xg + 32 * ig : isq ? 512 * xg + 32 * (ig - 4) : 1024 + 256 * xg + 32 * (ig - 20);
      const int rs = isq ? 8192 : 6144;
      const __amdgpu_buffer_rsrc_t rm = mkrs(isq ? wq : wmod, D * rs);
      v4u in8;
#pragma unroll
      for (int d = 0; d < 4; ++d) in8[d] = ((const GAS unsigned*)(isq ? xin : cin))[lane + 64 * d];
      const int vo = (4 * (lane >> 2)) * rs + (lane & 3) * 16;
      v4u R[32];
#pragma unroll
      for (int u = 0; u < 32; ++u) R[u] = ldnt(rm, vo, (64 * (u >> 2) + (u & 3)) * rs + c0 * 2);
      if (ig < 20) {
        const v4u s4 = {SENT, SENT, SENT, SENT};
        const int co = ig < 4 ? BY_OFF + (4 * xg + ig) * 5120 : AO_OFF + xg * AGRP + (ig - 4) * 5120;
#pragma unroll
        for (int i = 0; i < 5; ++i) stsc(s4, rws, lane * 16, co + i * 1024);
      }
      SCHED();
      float xv[8];
#pragma unroll
      for (int d = 0; d < 4; ++d) { xv[2 * d] = lo16(in8[d]); xv[2 * d + 1] = hi16(in8[d]); }
      if (isq) {
        const float mean = wave_sum(((xv[0] + xv[1]) + (xv[2] + xv[3])) + ((xv[4] + xv[5]) + (xv[6] + xv[7]))) * (1.0f / D);
        float sq = 0.f;
#pragma unroll
        for (int e = 0; e < 8; ++e) sq += (xv[e] - mean) * (xv[e] - mean);
        const float var = rbf(wave_sum(sq) * (1.0f / D));
        const float mb = rbf(mean), isd = rcp(rbf(__builtin_amdgcn_sqrtf(rbf(var + 1e-5f))));
#pragma unroll
        for (int e = 0; e < 8; ++e) xv[e] = rbf(rbf(xv[e] - mb) * isd);
      } else {
#pragma unroll
        for (int e = 0; e < 8; ++e) xv[e] = silu(xv[e]);
      }
#pragma unroll
      for (int p = 0; p < 16; ++p) {
        const v4u ra = R[2 * p], rb = R[2 * p + 1];
#pragma unroll
        for (int d = 0; d < 4; ++d) { R[2 * p][d] = __builtin_amdgcn_perm(rb[d], ra[d], PLO); R[2 * p + 1][d] = __builtin_amdgcn_perm(rb[d], ra[d], PHI); }
        asm volatile("" : "+v"(R[2 * p]), "+v"(R[2 * p + 1]));
      }
      VM0();
      STAMP(2);
      if (isq) {
        const gu64* gs = (const gu64*)(ws + MODG_OFF) + lane;
        u64 v[8];
        for (unsigned spins = 0;; ++spins) {
          bool ok = true;
#pragma unroll
          for (int d = 0; d < 8; ++d) v[d] = __hip_atomic_load((gu64*)gs + 64 * d, RLX_AGENT);
#pragma unroll
          for (int d = 0; d < 8; ++d) ok &= (unsigned)(v[d] >> 32) == tag;
          if (__all(ok) || spins > SPIN_CAP) break;
          __builtin_amdgcn_s_sleep(SLP1);
        }
        STAMP(3);
#pragma unroll
        for (int d = 0; d < 4; ++d) {
          const unsigned sa = (unsigned)v[d], ca = (unsigned)v[4 + d];
          xv[2 * d] = rbf(xv[2 * d] * rbf(1.0f + lo16(ca))) + lo16(sa);
          xv[2 * d + 1] = rbf(xv[2 * d + 1] * rbf(1.0f + hi16(ca))) + hi16(sa);
        }
      }
#pragma unroll
      for (int d = 0; d < 4; ++d) ((LAS unsigned*)(L + L_ACT))[lane + 64 * d] = cvtpk(xv[2 * d], xv[2 * d + 1]);
      CBAR();
      float acc[8];
#pragma unroll
      for (int e = 0; e < 8; ++e) acc[e] = 0.f;
#pragma unroll
      for (int k = 0; k < 8; ++k) {
        const v2u ap = *(const LAS v2u*)(L + L_ACT + k * 128 + (lane >> 2) * 8);
#pragma unroll
        for (int d = 0; d < 4; ++d) {
          acc[2 * d] = dot2(R[4 * k + 2][d], ap.y, dot2(R[4 * k][d], ap.x, acc[2 * d]));
          acc[2 * d + 1] = dot2(R[4 * k + 3][d], ap.y, dot2(R[4 * k + 1][d], ap.x, acc[2 * d + 1]));
        }
      }
      const float t0 = sum_b23(row_tr(acc[0], acc[2], acc[4], acc[6])), t1 = sum_b23(row_tr(acc[1], acc[3], acc[5], acc[7]));
      if (n < 4) __hip_atomic_store((gu64*)(ws + (isq ? QG_OFF : MODG_OFF)) + (c0 >> 1) + 4 * n + g, ((u64)tag << 32) | cvtpk(t0, t1), RLX_AGENT);
      STAMP(isq ? 5 : 4);
    }
    {
      const gu64* gs = (const gu64*)(ws + QG_OFF) + 256 * xg + lane;
      u64 v[4];
      for (unsigned spins = 0;; ++spins) {
        bool ok = true;
#pragma unroll
        for (int d = 0; d < 4; ++d) v[d] = __hip_atomic_load((gu64*)gs + 64 * d, RLX_AGENT);
#pragma unroll
        for (int d = 0; d < 4; ++d) ok &= (unsigned)(v[d] >> 32) == tag;
        if (__all(ok) || spins > SPIN_CAP) break;
        __builtin_amdgcn_s_sleep(SLP2);
      }
#pragma unroll
      for (int d = 0; d < 4; ++d) ((LAS unsigned*)(L + L_Q))[64 * d + lane] = (unsigned)v[d];
      LGKM0();
      ctl[0] = 1u;
      STAMP(6);
    }
    __builtin_amdgcn_s_setprio(0);
    if (isC2 || isC3) {
      const int ro = lane >> 2, c4 = lane & 3;
#pragma unroll 1
      for (int k = 0; k < 8; ++k) {
        const bool isw1 = isC3 && k < 4;
        const __amdgpu_buffer_rsrc_t r = mkrs(isC2 ? wo : (isw1 ? w1 : w2), isC2 ? 4096 * D * 2 : D * FF * 2);
        const int rs = isw1 ? FF * 2 : D * 2;
        const int vo = isw1 ? (8 * ro) * (FF * 2) + c4 * 16 : (8 * g) * (D * 2) + n * 16;
        const int so = isC2 ? (256 * hp + 32 * k) * (D * 2) + cq * 256 : (isw1 ? 128 * k * (FF * 2) + j3 * 64 : 32 * j3 * (D * 2) + (k - 4) * 256);
        const int la = isC2 ? (((n >> 1) * 8 + k) * 64 + g * 16 + 8 * (n & 1)) * 16
                            : (isw1 ? (((4 * k + (ro >> 2)) * 2 + (c4 >> 1)) * 64 + (ro & 3) * 16 + 8 * (c4 & 1)) * 16
                                    : 32768 + ((8 * (k - 4) + (n >> 1)) * 64 + g * 16 + 8 * (n & 1)) * 16);
        v4u R[8];
#pragma unroll
        for (int u = 0; u < 8; ++u) R[u] = ldnt(r, vo, so + u * rs);
        SCHED();
#pragma unroll
        for (int e = 0; e < 8; ++e) {
          v4u u;
#pragma unroll
          for (int jj = 0; jj < 4; ++jj) u[jj] = __builtin_amdgcn_perm(R[2 * jj + 1][e >> 1], R[2 * jj][e >> 1], (e & 1) ? PHI : PLO);
          *(LAS v4u*)(L + L_W + la + e * 16) = u;
        }
      }
    }
    if (isC3) {
      const gu64* gm = (const gu64*)(ws + MODG_OFF) + 512 + lane;
      const v4u x8 = *(const GAS v4u*)((const GAS u16*)xin + 8 * lane);
      u64 t[16];
      for (unsigned spins = 0;; ++spins) {
        bool ok = true;
#pragma unroll
        for (int d = 0; d < 16; ++d) t[d] = __hip_atomic_load((gu64*)gm + 64 * d, RLX_AGENT);
#pragma unroll
        for (int d = 0; d < 16; ++d) ok &= (unsigned)(t[d] >> 32) == tag;
        if (__all(ok) || spins > SPIN_CAP) break;
      }
#pragma unroll
      for (int d = 0; d < 16; ++d) ((LAS unsigned*)(L + L_MOD))[64 * d + lane] = (unsigned)t[d];
      *(LAS v4u*)(L + L_MOD + 4096 + lane * 16) = x8;
      ctl[1] = tag;
    }
    *(LAS f32x4*)(L + L_MO + lane * 32) = f32x4{0.f, 0.f, 0.f, 0.f};
    *(LAS f32x4*)(L + L_MO + lane * 32 + 16) = f32x4{0.f, 0.f, 0.f, 0.f};
    if (lane < 4) { ((LAS float*)(L + L_MM))[lane * 8] = -1e30f; ((LAS float*)(L + L_ML))[lane * 8] = 0.f; }
    STAMP(7);
  } else {
    INDICES();
    constexpr float C2 = 0.088388347648318440f * 1.4426950408889634f;
    const int nt = ig < 16 ? T_A : ig < 24 ? T_B : T_C2;
    const int t0 = nt * ig + (ig < 16 ? 0 : ig < 24 ? 16 * (T_A - T_B) : 2048 - 32 * T_C2);
    const __amdgpu_buffer_rsrc_t rk = mkrs(kc + ((size_t)xg * SK + (size_t)t0 * 32) * DH, nt * 8192);
    const __amdgpu_buffer_rsrc_t rv = mkrs(vc + ((size_t)xg * SK + (size_t)t0 * 32) * DH, nt * 8192);
    int koff = (4 * (n & 3) + ((n >> 2) & 1)) * 256 + (n >> 3) * 64 + 16 * g, voff = lane * 16;
    v4u kf[3][8], vr[3][8];
    int id = w - 1;
#define FETCH(d, dist) { \
      for (unsigned spins = 0; (int)__builtin_amdgcn_readfirstlane(ctl[2]) < id - (dist) + 1 && spins < (1u << 22); ++spins) __builtin_amdgcn_s_sleep(1); \
      const int so_ = id * 8192; asm volatile("" : "+v"(koff), "+v"(voff) :: "memory"); \
      _Pragma("unroll") for (int u = 0; u < 8; ++u) kf[d][u] = ldnt(rk, koff + ((u >> 1) & 1) * 512 + (u & 1) * 128, so_ + (u >> 2) * 4096); \
      _Pragma("unroll") for (int u = 0; u < 8; ++u) vr[d][u] = ldnt(rv, voff + (u & 3) * 1024, so_ + (u >> 2) * 4096); \
      SCHED(); \
      asm volatile("s_waitcnt vmcnt(%0)" ::"i"(SIGK) : "memory"); \
      if (lane == 0) __hip_atomic_fetch_add((LAS unsigned*)(L + L_CTL + 8), 1u, __ATOMIC_RELAXED, __HIP_MEMORY_SCOPE_WORKGROUP); \
      STAMP(32 + id); \
      SCHED(); \
      id += 7; }
#define COMPUTE(d) { \
      f32x4 sq[4]; \
      _Pragma("unroll") for (int t = 0; t < 4; ++t) sq[t] = mfma(kf[d][2 * t + 1], qf[1], mfma(kf[d][2 * t], qf[0], f32x4{0.f, 0.f, 0.f, 0.f})); \
      float sc[2][4]; \
      _Pragma("unroll") for (int j = 0; j < 8; ++j) { \
        auto x_ = __builtin_amdgcn_permlane32_swap(__float_as_uint(sq[2 * (j >> 2)][j & 3]), __float_as_uint(sq[2 * (j >> 2) + 1][j & 3]), false, false); \
        sc[j >> 2][j & 3] = __uint_as_float(x_[0]) + dpp<ROR4>(__uint_as_float(x_[1])); } \
      float tm = fmaxf(fmaxf(fmaxf(sc[0][0], sc[0][1]), fmaxf(sc[0][2], sc[0][3])), fmaxf(fmaxf(sc[1][0], sc[1][1]), fmaxf(sc[1][2], sc[1][3]))); \
      tm = max32(max16(tm)); \
      const float mn = fmaxf(m, tm * C2); \
      const float alpha = ex2(m - mn); \
      m = mn; \
      float p[8]; \
      _Pragma("unroll") for (int j = 0; j < 8; ++j) p[j] = ex2(sc[j >> 2][j & 3] * C2 - mn); \
      l = l * alpha + (((p[0] + p[1]) + (p[2] + p[3])) + ((p[4] + p[5]) + (p[6] + p[7]))); \
      const v4u pf = {cvtpk(p[0], p[1]), cvtpk(p[2], p[3]), cvtpk(p[4], p[5]), cvtpk(p[6], p[7])}; \
      const float a0 = dpp<0x150>(alpha), a1 = dpp<0x151>(alpha), a2 = dpp<0x152>(alpha), a3 = dpp<0x153>(alpha); \
      _Pragma("unroll") for (int e = 0; e < 8; ++e) { \
        acc[e][0] *= a0; acc[e][1] *= a1; acc[e][2] *= a2; acc[e][3] *= a3; \
        v4u bf; \
        _Pragma("unroll") for (int jj = 0; jj < 4; ++jj) \
          bf[jj] = __builtin_amdgcn_perm(vr[d][2 * jj + 1][e >> 1], vr[d][2 * jj][e >> 1], (e & 1) ? PHI : PLO); \
        acc[e] = mfma(pf, bf, acc[e]); } \
      SCHED(); }
    FETCH(2, DIST0) FETCH(1, DIST0) FETCH(0, DIST0)
    for (unsigned spins = 0; __builtin_amdgcn_readfirstlane(ctl[0]) == 0u && spins < (1u << 22); ++spins) __builtin_amdgcn_s_sleep(1);
    CBAR();
    v4u qf[2];
#pragma unroll
    for (int s = 0; s < 2; ++s) qf[s] = *(const LAS v4u*)(L + L_Q + (n & 3) * 256 + 128 * s + 64 * ((n >> 2) & 1) + 16 * g);
    f32x4 acc[8];
#pragma unroll
    for (int e = 0; e < 8; ++e) acc[e] = f32x4{0.f, 0.f, 0.f, 0.f};
    float m = -1e30f, l = 0.f;
    if (w == 1) STAMP(8);
    COMPUTE(2) COMPUTE(1)
#pragma unroll 1
    for (;;) {
      COMPUTE(0)
      if (id >= nt) break;
      FETCH(0, (id >= nt - KEND ? DISTE : DIST))
    }
    l = sum32(sum16(l));
    if (g == 0) {
#pragma unroll
      for (int r = 0; r < 4; ++r) {
        *(LAS f32x4*)(L + L_MO + ((w * 4 + r) * 128 + 8 * n) * 4) = f32x4{acc[0][r], acc[1][r], acc[2][r], acc[3][r]};
        *(LAS f32x4*)(L + L_MO + ((w * 4 + r) * 128 + 8 * n + 4) * 4) = f32x4{acc[4][r], acc[5][r], acc[6][r], acc[7][r]};
      }
      if (n < 4) { ((LAS float*)(L + L_MM))[n * 8 + w] = m; ((LAS float*)(L + L_ML))[n * 8 + w] = l; }
    }
  }
  int pass = (DRY && w_0 == 0) ? 0 : 1;
  for (;;) {
    pass = __builtin_amdgcn_readfirstlane(pass);
    asm volatile("" : "+s"(pass));
#define PBAR() do { LGKM0(); if (pass) __builtin_amdgcn_s_barrier(); CBAR(); } while (0)
    INDICES();
    PBAR();
    if (w == 0 && pass) STAMP(10);
    {
      const int r = w >> 1, sub = lane & 3, dc = (w & 1) * 16 + (lane >> 2);
      const f32x4 ma = *(const LAS f32x4*)(L + L_MM + r * 32), mb = *(const LAS f32x4*)(L + L_MM + r * 32 + 16);
      const f32x2 mo = *(const LAS f32x2*)(L + L_MM + r * 32 + sub * 8), lo = *(const LAS f32x2*)(L + L_ML + r * 32 + sub * 8);
      const f32x4 oa = *(const LAS f32x4*)(L + L_MO + (((2 * sub) * 4 + r) * 128 + 4 * dc) * 4), ob = *(const LAS f32x4*)(L + L_MO + (((2 * sub + 1) * 4 + r) * 128 + 4 * dc) * 4);
      const float M = fmaxf(fmaxf(fmaxf(ma[0], ma[1]), fmaxf(ma[2], ma[3])), fmaxf(fmaxf(mb[0], mb[1]), fmaxf(mb[2], mb[3])));
      const float wa = ex2(mo.x - M), wb = ex2(mo.y - M);
      const float Ls = sum_q(wa * lo.x + wb * lo.y);
      f32x4 o = wa * oa + wb * ob;
#pragma unroll
      for (int e = 0; e < 4; ++e) o[e] = sum_q(o[e]);
      if (pass) {
        const int rec = AO_OFF + xg * AGRP + ((r >> 1) * 32 + ig) * AREC;
        stsc(asu(o), rws, sub == 0 ? (lane >> 2) * 16 : OOB, rec + (w & 3) * 256);
        __builtin_amdgcn_raw_buffer_store_b64(v2u{__float_as_uint(M), __float_as_uint(Ls)}, rws, (lane == 0 && !(w & 1)) ? 0 : OOB, rec + 1024 + (r & 1) * 8, AUX_SC1);
        if (w == 0) STAMP(11);
      }
    }
    if (isC2) {
      v4u wf[8];
#pragma unroll
      for (int k = 0; k < 8; ++k) wf[k] = *(const LAS v4u*)(L + L_W + ((w * 8 + k) * 64 + lane) * 16);
      const int sg = lane >> 3, dc = lane & 7;
      const int base = AO_OFF + xg * AGRP + (hp & 1) * 32 * AREC;
      int aw = L_ACT + (w * 32 + dc * 4 + g) * 2, ar = L_ACT + g * 16, sv = n == 0 ? g * 16 : OOB;
      asm volatile("" : "+v"(aw), "+v"(ar), "+v"(sv));
      v4u o4[4] = {}; v2u ml[4] = {};
      for (unsigned spins = 0; pass; ++spins) {
        CBAR();
#pragma unroll
        for (int i = 0; i < 4; ++i) ml[i] = __builtin_amdgcn_raw_buffer_load_b64(rws, sg * AREC, base + i * 8 * AREC + 1024 + (w >> 2) * 8, AUX_SC1);
#pragma unroll
        for (int i = 0; i < 4; ++i) o4[i] = ldsc(rws, sg * AREC + dc * 16, base + i * 8 * AREC + w * 128);
        const unsigned mx = umax(umax(umax(umax4(o4[0]), umax4(o4[1])), umax(umax4(o4[2]), umax4(o4[3]))),
                                 umax(umax(umax(ml[0].x, ml[0].y), umax(ml[1].x, ml[1].y)), umax(umax(ml[2].x, ml[2].y), umax(ml[3].x, ml[3].y))));
        if (!__any(mx == SENT) || spins > SPIN_CAP) break;
      }
      if (w == 0 && pass) STAMP(12);
      float M = fmaxf(fmaxf(__uint_as_float(ml[0].x), __uint_as_float(ml[1].x)), fmaxf(__uint_as_float(ml[2].x), __uint_as_float(ml[3].x)));
      M = fmaxf(M, dpp<ROR8>(M)); M = max32(max16(M));
      float wt[4];
#pragma unroll
      for (int i = 0; i < 4; ++i) wt[i] = ex2(__uint_as_float(ml[i].x) - M);
      float Lt = (wt[0] * __uint_as_float(ml[0].y) + wt[1] * __uint_as_float(ml[1].y)) + (wt[2] * __uint_as_float(ml[2].y) + wt[3] * __uint_as_float(ml[3].y));
      Lt += dpp<ROR8>(Lt); Lt = sum32(sum16(Lt));
      const f32x4 o = (wt[0] * asf(o4[0]) + wt[1] * asf(o4[1])) + (wt[2] * asf(o4[2]) + wt[3] * asf(o4[3]));
      float ot = row_tr(o[0], o[1], o[2], o[3]);
      ot = (ot + dpp<ROR8>(ot)) * rcp(Lt);
      *(LAS u16*)(L + aw) = (u16)cvtpk(ot, ot);
      PBAR();
      f32x4 y0 = {0.f, 0.f, 0.f, 0.f}, y1 = {0.f, 0.f, 0.f, 0.f};
#pragma unroll
      for (int k = 0; k < 8; k += 2) {
        y0 = mfma(wf[k], *(const LAS v4u*)(L + ar + k * 64), y0);
        y1 = mfma(wf[k + 1], *(const LAS v4u*)(L + ar + (k + 1) * 64), y1);
      }
      y0 += y1;
      asm volatile("" : "+v"(y0));
      if (pass) {
        stsc(asu(y0), rws, sv, BY_OFF + hp * 2048 + cq * 512 + w * 64);
        if (w == 0) STAMP(13);
      }
    } else if (isC3) {
      v4u f1[4], f2[4];
#pragma unroll
      for (int k = 0; k < 4; ++k) f1[k] = *(const LAS v4u*)(L + L_W + ((w * 4 + k) * 64 + lane) * 16);
#pragma unroll
      for (int k = 0; k < 4; ++k) f2[k] = *(const LAS v4u*)(L + L_W + 32768 + ((w * 4 + k) * 64 + lane) * 16);
      const int col = 64 * w + 4 * n + g;
      const LAS u16* md = (const LAS u16*)(L + L_MOD);
      float ga = lo16(md[col]), sb = lo16(md[512 + col]), cb = rbf(1.0f + lo16(md[1024 + col])), xv = lo16(md[2048 + col]);
      int a_x1 = L_X1 + col * 4, a_st = L_ST + w * 8 + (lane >> 5) * 4, a_hw = L_ACT + col * 2, a_hr = L_ACT + w * 128 + g * 16, a_rw = L_RED + w * 128 + g * 16,
          a_rr = L_RED + (lane >> 5) * 512 + (lane & 31) * 4, a_1w = L_H1 + w * 64 + (lane & 31) * 2, a_1r = L_H1 + w * 64 + g * 16, sv = n == 0 ? g * 16 : OOB;
      asm volatile("" : "+v"(ga), "+v"(sb), "+v"(cb), "+v"(xv), "+v"(a_x1), "+v"(a_st), "+v"(a_hw), "+v"(a_hr), "+v"(a_rw), "+v"(a_rr), "+v"(a_1w), "+v"(a_1r), "+v"(sv));
      v4u yp[4] = {};
      for (unsigned spins = 0; pass; ++spins) {
        CBAR();
#pragma unroll
        for (int i = 0; i < 4; ++i) yp[i] = ldsc(rws, g * 2048 + n * 16, BY_OFF + i * 8192 + w * 256);
        if (!__any(umax(umax(umax4(yp[0]), umax4(yp[1])), umax(umax4(yp[2]), umax4(yp[3]))) == SENT) || spins > SPIN_CAP) break;
      }
      if (w == 0 && pass) STAMP(14);
      const f32x4 ys = (asf(yp[0]) + asf(yp[1])) + (asf(yp[2]) + asf(yp[3]));
      const float x1 = rbf(xv + rbf(ga * rbf(row_tr(ys[0], ys[1], ys[2], ys[3]))));
      *(LAS float*)(L + a_x1) = x1;
      *(LAS float*)(L + a_st) = sum_row(sum16(tr32(x1, x1 * x1)));
      PBAR();
      {
        const f32x4 t = (*(const LAS f32x4*)(L + L_ST) + *(const LAS f32x4*)(L + L_ST + 16)) + (*(const LAS f32x4*)(L + L_ST + 32) + *(const LAS f32x4*)(L + L_ST + 48));
        const float mean = (t[0] + t[2]) * (1.0f / D);
        const float var = rbf(fmaxf((t[1] + t[3]) * (1.0f / D) - mean * mean, 0.f));
        const float isd = rcp(rbf(__builtin_amdgcn_sqrtf(rbf(var + 1e-5f))));
        const float h = rbf(rbf(rbf(x1 - rbf(mean)) * isd) * cb) + sb;
        *(LAS u16*)(L + a_hw) = (u16)cvtpk(h, h);
        CBAR();
        const v4u a0 = *(const LAS v4u*)(L + a_hr), a1 = *(const LAS v4u*)(L + a_hr + 64);
        const f32x4 z = {0.f, 0.f, 0.f, 0.f};
        const f32x4 t0 = mfma(f1[2], a1, mfma(f1[0], a0, z)), t1 = mfma(f1[3], a1, mfma(f1[1], a0, z));
        *(LAS f32x4*)(L + a_rw) = t0;
        *(LAS f32x4*)(L + a_rw + 64) = t1;
      }
      PBAR();
      {
        const LAS float* rr = (const LAS float*)(L + a_rr);
        const float hv = silu(rbf(sum32((rr[0] + rr[32]) + (rr[64] + rr[96]))));
        *(LAS u16*)(L + a_1w) = (u16)cvtpk(hv, hv);
        CBAR();
        const v4u a = *(const LAS v4u*)(L + a_1r);
        const f32x4 z = {0.f, 0.f, 0.f, 0.f};
        f32x4 t[4];
#pragma unroll
        for (int k = 0; k < 4; ++k) t[k] = mfma(f2[k], a, z);
        asm volatile("" : "+v"(t[0]), "+v"(t[1]), "+v"(t[2]), "+v"(t[3]));
        if (pass) {
#pragma unroll
          for (int k = 0; k < 4; ++k) stsc(asu(t[k]), rws, sv + k * 64, CO_OFF + j3 * 2048 + w * 256);
          if (w == 0) STAMP(15);
        }
      }
      if (j3 < 32 && w == 0) {
        const __amdgpu_buffer_rsrc_t rc = mkrs(ws + CO_OFF, 56 * 2048);
        const int oc = 16 * j3 + 4 * (lane & 3) + g;
        const float xr = ((const LAS float*)(L + L_X1))[oc], gr = lo16(md[1536 + oc]);
        v4u cp[4] = {};
        for (unsigned spins = 0; pass; ++spins) {
          CBAR();
#pragma unroll
          for (int i = 0; i < 4; ++i) cp[i] = ldsc(rc, (lane >> 2) * 2048 + (lane & 3) * 16, i * 32768 + j3 * 64);
          if (!__any(umax(umax(umax4(cp[0]), umax4(cp[1])), umax(umax4(cp[2]), umax4(cp[3]))) == SENT) || spins > SPIN_CAP) break;
        }
        if (pass) STAMP(16);
        const f32x4 cs = (asf(cp[0]) + asf(cp[1])) + (asf(cp[2]) + asf(cp[3]));
        const float o = xr + rbf(gr * rbf(sum_b23(row_tr(cs[0], cs[1], cs[2], cs[3]))));
        unsigned ob = cvtpk(o, o);
        asm volatile("" : "+v"(ob));
        if (pass) {
          if (n < 4) ((GAS u16*)out)[oc] = (u16)ob;
          if (j3 == 0 && lane == 0) __hip_atomic_store((gu32*)(ws + EPOCH_OFF), ctl[1], RLX_AGENT);
          STAMP(17);
        }
      }
    }
    if (pass) break;
    pass = 1;
    STAMP(18);
  }
}

extern "C" void kernel_launch(void* const* d_in, const int* in_sizes, int n_in, void* d_out, int out_size, void* d_ws, size_t ws_size,
                              hipStream_t stream) {
  (void)in_sizes; (void)n_in; (void)out_size; (void)ws_size;
  (void)hipFuncSetAttribute((const void*)fused_decode, hipFuncAttributeMaxDynamicSharedMemorySize, L_BYTES);
  fused_decode<<<NWG, NTHR, L_BYTES, stream>>>((const u16*)d_in[0], (const u16*)d_in[1], (const u16*)d_in[2], (const u16*)d_in[3], (const u16*)d_in[4],
                                               (const u16*)d_in[6], (const u16*)d_in[7], (const u16*)d_in[8], (const u16*)d_in[9], (u16*)d_out,
                                               (unsigned char*)d_ws);
}
```

```cpp
#include <hip/hip_runtime.h>
#include <stdint.h>

#define GAS __attribute__((address_space(1)))
#define LAS __attribute__((address_space(3)))
typedef unsigned v4u __attribute__((ext_vector_type(4)));
typedef unsigned v2u __attribute__((ext_vector_type(2)));
typedef float f32x4 __attribute__((ext_vector_type(4)));
typedef float f32x2 __attribute__((ext_vector_type(2)));
typedef short bf16x8 __attribute__((ext_vector_type(8)));
typedef __bf16 bf16x2 __attribute__((ext_vector_type(2)));
typedef unsigned short u16;
typedef unsigned long long u64;
typedef GAS unsigned gu32;
typedef GAS u64 gu64;
#define RLX_AGENT __ATOMIC_RELAXED, __HIP_MEMORY_SCOPE_AGENT

#define DIST0 3
#define DIST 2
#define DISTE 3
#define KEND 16
#define SIGK 8
#define SLP1 4
#define SLP2 8
#define E1 1
#define E2 0
#define E3 1
#define SIGPRE 1
#define PACE 0
#define C2LEAD 2
#define PBIAS 0.5f
#define PPRE 0
#define PFIRST 22
#define PSTEP 8
#define PLAST 6
#define PTAU 57
#define NPEER 32
#define PGAIN 2.5f
#define DRY 1
#define T_P1 65
#define T_P2 61
#define T_NU 64
#define T_PL 69
#define T_C3 63
#define T_C2 60
static_assert(4 * T_P1 + 8 * T_P2 + 4 * T_NU + 8 * T_PL + 4 * T_C3 + 4 * T_C2 == 2048, "tiles of one kv head");

constexpr int D = 512, SK = 65536, DH = 128, FF = 1792;
constexpr int NWG = 256, NW = 8, NTHR = NW * 64;
constexpr int AUX_NT = 2, AUX_SC1 = 16;
constexpr unsigned SENT = 0xFFFFFFFFu;
constexpr unsigned SPIN_CAP = 1u << 16;
constexpr int OOB = 0x40000000;
constexpr int EPOCH_OFF = 0;
constexpr int PRG_OFF = 32768;
constexpr int MODG_OFF = 4096;
constexpr int QG_OFF = 16384;
constexpr int AO_OFF = 65536;
constexpr int AML_OFF = AO_OFF + 256 * 1024;
constexpr int BY_OFF = AML_OFF + 256 * 1024;
constexpr int CO_OFF = BY_OFF + 32 * 1024;
constexpr int WS_BYTES = CO_OFF + 28 * 2048;
constexpr int STAMP_OFF = 16 << 20;
constexpr int L_W = 0;
constexpr int L_MO = 131072;
constexpr int L_MM = L_MO + 16384;
constexpr int L_ML = L_MM + 128;
constexpr int L_Q = L_ML + 128;
constexpr int L_ACT = L_Q + 1024;
constexpr int L_RED = L_ACT + 1024;
constexpr int L_ST = L_RED + 4096;
constexpr int L_H1 = L_ST + 64;
constexpr int L_X1 = L_H1 + 1024;
constexpr int L_MOD = L_X1 + 2048;
constexpr int L_CTL = L_MOD + 5120;
constexpr int L_STAMP = L_CTL + 16;
constexpr int L_BYTES = L_STAMP;
static_assert(L_BYTES <= 160 * 1024, "LDS");

__device__ __forceinline__ float lo16(unsigned w) { return __builtin_bit_cast(float, w << 16); }
__device__ __forceinline__ float hi16(unsigned w) { return __builtin_bit_cast(float, w & 0xffff0000u); }
__device__ __forceinline__ unsigned cvtpk(float lo, float hi) {
  bf16x2 r; r.x = (__bf16)lo; r.y = (__bf16)hi;
  return __builtin_bit_cast(unsigned, r);
}
#define rbf(x) __builtin_bit_cast(float, cvtpk(zero_, (x)))
__device__ __forceinline__ f32x4 mfma(v4u a, v4u b, f32x4 c) {
  return __builtin_amdgcn_mfma_f32_16x16x32_bf16(__builtin_bit_cast(bf16x8, a), __builtin_bit_cast(bf16x8, b), c, 0, 0, 0);
}
__device__ __forceinline__ float dot2(unsigned a, unsigned b, float acc) {
  return __builtin_amdgcn_fdot2_f32_bf16(__builtin_bit_cast(bf16x2, a), __builtin_bit_cast(bf16x2, b), acc, false);
}
__device__ __forceinline__ float rcp(float x) { return __builtin_amdgcn_rcpf(x); }
__device__ __forceinline__ float ex2(float x) { return __builtin_amdgcn_exp2f(x); }
__device__ __forceinline__ float silu(float x) { return x * rcp(1.0f + ex2(x * -1.4426950408889634f)); }
template <int CTRL> __device__ __forceinline__ float dpp(float x) {
  return __builtin_bit_cast(float, __builtin_amdgcn_mov_dpp(__builtin_bit_cast(int, x), CTRL, 0xf, 0xf, true));
}
constexpr int XOR1 = 0xB1, XOR2 = 0x4E, HMIR = 0x141, ROR4 = 0x124, ROR8 = 0x128;
__device__ __forceinline__ float sum16(float x) {
  auto s = __builtin_amdgcn_permlane16_swap(__float_as_uint(x), __float_as_uint(x), false, false);
  return __uint_as_float(s[0]) + __uint_as_float(s[1]);
}
__device__ __forceinline__ float sum32(float x) {
  auto s = __builtin_amdgcn_permlane32_swap(__float_as_uint(x), __float_as_uint(x), false, false);
  return __uint_as_float(s[0]) + __uint_as_float(s[1]);
}
__device__ __forceinline__ float max16(float x) {
  auto s = __builtin_amdgcn_permlane16_swap(__float_as_uint(x), __float_as_uint(x), false, false);
  return fmaxf(__uint_as_float(s[0]), __uint_as_float(s[1]));
}
__device__ __forceinline__ float max32(float x) {
  auto s = __builtin_amdgcn_permlane32_swap(__float_as_uint(x), __float_as_uint(x), false, false);
  return fmaxf(__uint_as_float(s[0]), __uint_as_float(s[1]));
}
__device__ __forceinline__ float sum_q(float x) { x += dpp<XOR1>(x); x += dpp<XOR2>(x); return x; }
__device__ __forceinline__ float sum_8(float x) { x = sum_q(x); x += dpp<HMIR>(x); return x; }
__device__ __forceinline__ float sum_b23(float x) { x += dpp<ROR4>(x); x += dpp<ROR8>(x); return x; }
__device__ __forceinline__ float sum_row(float x) { return sum_b23(sum_q(x)); }
__device__ __forceinline__ float wave_sum(float x) { return sum32(sum16(sum_row(x))); }
__device__ __forceinline__ float quad_tr(f32x4 v, int lane) {
  const bool b0 = lane & 1, b1 = lane & 2;
  const float t0 = (b0 ? v[1] : v[0]) + dpp<XOR1>(b0 ? v[0] : v[1]);
  const float t1 = (b0 ? v[3] : v[2]) + dpp<XOR1>(b0 ? v[2] : v[3]);
  return (b1 ? t1 : t0) + dpp<XOR2>(b1 ? t0 : t1);
}
__device__ __forceinline__ float tr16(float a, float b) {
  auto s = __builtin_amdgcn_permlane16_swap(__float_as_uint(a), __float_as_uint(b), false, false);
  return __uint_as_float(s[0]) + __uint_as_float(s[1]);
}
__device__ __forceinline__ float tr32(float a, float b) {
  auto s = __builtin_amdgcn_permlane32_swap(__float_as_uint(a), __float_as_uint(b), false, false);
  return __uint_as_float(s[0]) + __uint_as_float(s[1]);
}
__device__ __forceinline__ float row_tr(float a, float b, float c, float d) { return tr32(tr16(a, b), tr16(c, d)); }
#define LGKM0() asm volatile("s_waitcnt lgkmcnt(0)" ::: "memory")
#define VM0() asm volatile("s_waitcnt vmcnt(0)" ::: "memory")
#define BAR() do { LGKM0(); __builtin_amdgcn_s_barrier(); asm volatile("" ::: "memory"); } while (0)
#define SCHED() __builtin_amdgcn_sched_barrier(0)
#define CBAR() asm volatile("" ::: "memory")
__device__ __forceinline__ unsigned umax(unsigned a, unsigned b) { return a > b ? a : b; }
__device__ __forceinline__ unsigned umax4(v4u v) { return umax(umax(v.x, v.y), umax(v.z, v.w)); }
__device__ __forceinline__ __amdgpu_buffer_rsrc_t mkrs(const void* p, int bytes) { return __builtin_amdgcn_make_buffer_rsrc((void*)p, 0, bytes, 0x00020000); }
__device__ __forceinline__ v4u ldnt(__amdgpu_buffer_rsrc_t r, int vo, int so) { return __builtin_amdgcn_raw_buffer_load_b128(r, vo, so, AUX_NT); }
__device__ __forceinline__ v4u ldsc(__amdgpu_buffer_rsrc_t r, int vo, int so) { return __builtin_amdgcn_raw_buffer_load_b128(r, vo, so, AUX_SC1); }
__device__ __forceinline__ void stsc(v4u v, __amdgpu_buffer_rsrc_t r, int vo, int so) { __builtin_amdgcn_raw_buffer_store_b128(v, r, vo, so, AUX_SC1); }
__device__ __forceinline__ f32x4 asf(v4u v) { return __builtin_bit_cast(f32x4, v); }
__device__ __forceinline__ v4u asu(f32x4 v) { return __builtin_bit_cast(v4u, v); }
#define PLO 0x05040100u
#define PHI 0x07060302u

#define STAMP(k) do {} while (0)

__global__ void __launch_bounds__(NTHR, 2)
fused_decode(const u16* __restrict__ xin, const u16* __restrict__ kc, const u16* __restrict__ vc, const u16* __restrict__ wq,
             const u16* __restrict__ wo, const u16* __restrict__ w1, const u16* __restrict__ w2, const u16* __restrict__ cin,
             const u16* __restrict__ wmod, u16* __restrict__ out, unsigned char* ws) {
  extern __shared__ __attribute__((aligned(16))) unsigned char smem[];
  LAS unsigned char* L = (LAS unsigned char*)smem;
  volatile LAS unsigned* ctl = (volatile LAS unsigned*)(L + L_CTL);
  const int w_0 = __builtin_amdgcn_readfirstlane(threadIdx.x >> 6);
#define INDICES() \
  int lane = threadIdx.x & 63, w = w_0, b = blockIdx.x; \
  asm volatile("" : "+v"(lane)); asm volatile("" : "+s"(w)); asm volatile("" : "+s"(b)); \
  float zero_ = 0.f; asm volatile("" : "+v"(zero_)); \
  const int g = lane >> 4, n = lane & 15, xg = b & 7, ig = b >> 3; \
  const bool isC2 = ig >= 28, isC3 = ig >= 24 && ig < 28 && xg < 7; \
  const int hp = 2 * xg + ((ig >> 1) & 1), cq = ig & 1;         \
  const int j3 = 4 * xg + (ig - 24);                            \
  (void)g; (void)n; (void)isC2; (void)isC3; (void)hp; (void)cq; (void)j3;
  asm volatile("" ::"s"(xin), "s"(kc), "s"(vc), "s"(wq), "s"(wo), "s"(w1), "s"(w2), "s"(cin), "s"(wmod), "s"(out), "s"(ws));
  const __amdgpu_buffer_rsrc_t rws = mkrs(ws, WS_BYTES);
  if (threadIdx.x == 0) { ctl[0] = 0u; ctl[2] = 0u; }
  BAR();
  LAS float* red = (LAS float*)(L + L_RED);
#define NTILES (ig < 4 ? T_P1 : ig < 12 ? T_P2 : ig < 16 ? T_NU : ig < 24 ? T_PL : ig < 28 ? T_C3 : T_C2)
#define TAG() (__hip_atomic_load((gu32*)(ws + EPOCH_OFF), RLX_AGENT) + 1u)
  {
    INDICES();
    if (w == 0) STAMP(1);
    const v4u s4 = {SENT, SENT, SENT, SENT};
    if (ig < 4) {
      const __amdgpu_buffer_rsrc_t rm = mkrs(wmod, D * 6144);
      const int c0 = 128 * xg + 32 * ig;
      v4u R[4];
#pragma unroll
      for (int j = 0; j < 4; ++j) R[j] = ldnt(rm, (4 * (lane >> 2)) * 6144 + (lane & 3) * 16, (64 * w + j) * 6144 + c0 * 2);
      const v2u c4 = *(const GAS v2u*)((const GAS u16*)cin + 64 * w + 4 * (lane >> 2));
      const unsigned tag = TAG();
      stsc(s4, rws, lane < 32 ? lane * 16 : OOB, BY_OFF + ((4 * xg + ig) * 8 + w) * 512);
      SCHED();
      const unsigned a0 = cvtpk(silu(lo16(c4.x)), silu(hi16(c4.x))), a1 = cvtpk(silu(lo16(c4.y)), silu(hi16(c4.y)));
      float acc[8];
#pragma unroll
      for (int d = 0; d < 4; ++d) {
        acc[2 * d] = dot2(__builtin_amdgcn_perm(R[3][d], R[2][d], PLO), a1, dot2(__builtin_amdgcn_perm(R[1][d], R[0][d], PLO), a0, 0.f));
        acc[2 * d + 1] = dot2(__builtin_amdgcn_perm(R[3][d], R[2][d], PHI), a1, dot2(__builtin_amdgcn_perm(R[1][d], R[0][d], PHI), a0, 0.f));
      }
      const float t0 = sum_b23(row_tr(acc[0], acc[2], acc[4], acc[6])), t1 = sum_b23(row_tr(acc[1], acc[3], acc[5], acc[7]));
      *(LAS f32x2*)(red + w * 32 + (n & 3) * 8 + g * 2) = f32x2{t0, t1};
      VM0();
      BAR();
      if (w == 0 && lane < 16) {
        f32x2 t = {0.f, 0.f};
#pragma unroll
        for (int i = 0; i < NW; ++i) t += *(const LAS f32x2*)(red + i * 32 + 2 * lane);
        __hip_atomic_store((gu64*)(ws + MODG_OFF) + (c0 >> 1) + lane, ((u64)tag << 32) | cvtpk(t.x, t.y), RLX_AGENT);
      }
      if (w == 0) STAMP(4);
    } else if (ig < 16) {
      const bool isq = ig < 12;
      const int c0 = isq ? 512 * xg + 64 * (ig - 4) : 1024 + 256 * xg + 64 * (ig - 12), rs = isq ? 8192 : 6144, r = lane >> 3;
      const __amdgpu_buffer_rsrc_t rm = mkrs(isq ? wq : wmod, D * rs);
      const v4u x8 = *(const GAS v4u*)((const GAS u16*)xin + 8 * lane);
      const unsigned xp = *(const GAS unsigned*)((const GAS u16*)(isq ? xin : cin) + 64 * w + 2 * (lane & 31));
      const unsigned tag = TAG();
      v4u R[8];
#pragma unroll
      for (int u = 0; u < 8; ++u) R[u] = ldnt(rm, (8 * r) * rs + (lane & 7) * 16, (64 * w + u) * rs + c0 * 2);
      stsc(s4, rws, (isq && lane < 32) ? lane * 16 : OOB, AO_OFF + xg * 32768 + ((ig - 4) * 8 + w) * 512);
      SCHED();
      float n0, n1;
      if (isq) {
        float xv[8];
#pragma unroll
        for (int d = 0; d < 4; ++d) { xv[2 * d] = lo16(x8[d]); xv[2 * d + 1] = hi16(x8[d]); }
        const float mean = wave_sum(((xv[0] + xv[1]) + (xv[2] + xv[3])) + ((xv[4] + xv[5]) + (xv[6] + xv[7]))) * (1.0f / D);
        float sq = 0.f;
#pragma unroll
        for (int e = 0; e < 8; ++e) sq += (xv[e] - mean) * (xv[e] - mean);
        const float var = rbf(wave_sum(sq) * (1.0f / D));
        const float mb = rbf(mean), isd = rcp(rbf(__builtin_amdgcn_sqrtf(rbf(var + 1e-5f))));
        n0 = rbf(rbf(lo16(xp) - mb) * isd); n1 = rbf(rbf(hi16(xp) - mb) * isd);
      } else {
        n0 = silu(lo16(xp)); n1 = silu(hi16(xp));
      }
#pragma unroll
      for (int p = 0; p < 4; ++p) {
        const v4u ra = R[2 * p], rb = R[2 * p + 1];
#pragma unroll
        for (int d = 0; d < 4; ++d) { R[2 * p][d] = __builtin_amdgcn_perm(rb[d], ra[d], PLO); R[2 * p + 1][d] = __builtin_amdgcn_perm(rb[d], ra[d], PHI); }
        asm volatile("" : "+v"(R[2 * p]), "+v"(R[2 * p + 1]));
      }
      asm volatile("" : "+v"(n0), "+v"(n1));
      VM0();
      if (w == 0) STAMP(2);
      if (isq) {
        u64 v;
        for (unsigned spins = 0;; ++spins) {
          v = __hip_atomic_load((gu64*)(ws + MODG_OFF) + (lane >> 5) * 256 + 32 * w + (lane & 31), RLX_AGENT);
          if (__all((unsigned)(v >> 32) == tag) || spins > SPIN_CAP) break;
        }
        if (w == 0) STAMP(3);
        const auto sc = __builtin_amdgcn_permlane32_swap((unsigned)v, (unsigned)v, false, false);
        n0 = rbf(n0 * rbf(1.0f + lo16(sc[1]))) + lo16(sc[0]); n1 = rbf(n1 * rbf(1.0f + hi16(sc[1]))) + hi16(sc[0]);
      }
      ((LAS unsigned*)(L + L_ACT))[32 * w + (lane & 31)] = cvtpk(n0, n1);
      CBAR();
      const v4u ap = *(const LAS v4u*)(L + L_ACT + 128 * w + 16 * r);
      float acc[8];
#pragma unroll
      for (int e = 0; e < 8; ++e) acc[e] = 0.f;
#pragma unroll
      for (int p = 0; p < 4; ++p)
#pragma unroll
        for (int d = 0; d < 4; ++d) { acc[2 * d] = dot2(R[2 * p][d], ap[p], acc[2 * d]); acc[2 * d + 1] = dot2(R[2 * p + 1][d], ap[p], acc[2 * d + 1]); }
      *(LAS f32x2*)(red + (w * 2 + (n >> 3)) * 64 + (n & 7) * 8 + g * 2) = f32x2{row_tr(acc[0], acc[2], acc[4], acc[6]), row_tr(acc[1], acc[3], acc[5], acc[7])};
      BAR();
      if (w == 0) {
        f32x2 t = {0.f, 0.f};
#pragma unroll
        for (int i = 0; i < NW; ++i) t += *(const LAS f32x2*)(red + ((lane >> 5) * 8 + i) * 64 + 2 * (lane & 31));
        t.x = sum32(t.x); t.y = sum32(t.y);
        if (lane < 32) __hip_atomic_store((gu64*)(ws + (isq ? QG_OFF : MODG_OFF)) + (c0 >> 1) + lane, ((u64)tag << 32) | cvtpk(t.x, t.y), RLX_AGENT);
        if (isq) STAMP(5);
      }
    } else if (isC2 || isC3) {
      const int ro = lane >> 3, c8 = lane & 7;
      v4u R[2][8];
#pragma unroll
      for (int h = 0; h < 2; ++h) {
        const __amdgpu_buffer_rsrc_t r = mkrs(isC2 ? wo : (h ? w2 : w1), isC2 ? 4096 * D * 2 : D * FF * 2);
        const int rs = (isC3 && !h) ? FF * 2 : D * 2;
        const int vo = isC2 ? (8 * g) * (D * 2) + n * 16 : (8 * ro) * rs + c8 * 16;
        const int so = isC2 ? (256 * hp + 32 * w) * (D * 2) + cq * 512 + h * 256 : (h ? 64 * j3 * (D * 2) + w * 128 : 64 * w * (FF * 2) + j3 * 128);
#pragma unroll
        for (int u = 0; u < 8; ++u) R[h][u] = ldnt(r, vo, so + u * rs);
      }
      SCHED();
      const int la = isC2 ? (((n >> 1) * 8 + w) * 64 + g * 16 + 8 * (n & 1)) * 16 : (((w * 2 + (ro >> 2)) * 4 + (c8 >> 1)) * 64 + (ro & 3) * 16 + 8 * (c8 & 1)) * 16;
#pragma unroll
      for (int h = 0; h < 2; ++h)
#pragma unroll
        for (int e = 0; e < 8; ++e) {
          v4u u;
#pragma unroll
          for (int jj = 0; jj < 4; ++jj) u[jj] = __builtin_amdgcn_perm(R[h][2 * jj + 1][e >> 1], R[h][2 * jj][e >> 1], (e & 1) ? PHI : PLO);
          *(LAS v4u*)(L + L_W + h * 65536 + la + e * 16) = u;
        }
    }
  }
  if (w_0 == 0) {
    INDICES();
    __builtin_amdgcn_s_setprio(3);
    const unsigned tag = TAG();
    {
      const gu64* gs = (const gu64*)(ws + QG_OFF) + 256 * xg + lane;
      u64 v[4];
      for (unsigned spins = 0;; ++spins) {
        bool ok = true;
#pragma unroll
        for (int d = 0; d < 4; ++d) v[d] = __hip_atomic_load((gu64*)gs + 64 * d, RLX_AGENT);
#pragma unroll
        for (int d = 0; d < 4; ++d) ok &= (unsigned)(v[d] >> 32) == tag;
        if (__all(ok) || spins > SPIN_CAP) break;
        __builtin_amdgcn_s_sleep(SLP2);
      }
#pragma unroll
      for (int d = 0; d < 4; ++d) ((LAS unsigned*)(L + L_Q))[64 * d + lane] = (unsigned)v[d];
      ctl[1] = tag;
      LGKM0();
      ctl[0] = 1u;
      STAMP(6);
    }
    __builtin_amdgcn_s_setprio(0);
    if (isC3) {
      const gu64* gm = (const gu64*)(ws + MODG_OFF) + 512 + lane;
      const v4u x8 = *(const GAS v4u*)((const GAS u16*)xin + 8 * lane);
      u64 t[16];
      for (unsigned spins = 0;; ++spins) {
        bool ok = true;
#pragma unroll
        for (int d = 0; d < 16; ++d) t[d] = __hip_atomic_load((gu64*)gm + 64 * d, RLX_AGENT);
#pragma unroll
        for (int d = 0; d < 16; ++d) ok &= (unsigned)(t[d] >> 32) == tag;
        if (__all(ok) || spins > SPIN_CAP) break;
      }
#pragma unroll
      for (int d = 0; d < 16; ++d) ((LAS unsigned*)(L + L_MOD))[64 * d + lane] = (unsigned)t[d];
      *(LAS v4u*)(L + L_MOD + 4096 + lane * 16) = x8;
    }
    *(LAS f32x4*)(L + L_MO + lane * 32) = f32x4{0.f, 0.f, 0.f, 0.f};
    *(LAS f32x4*)(L + L_MO + lane * 32 + 16) = f32x4{0.f, 0.f, 0.f, 0.f};
    if (lane < 4) { ((LAS float*)(L + L_MM))[lane * 8] = -1e30f; ((LAS float*)(L + L_ML))[lane * 8] = 0.f; }
    STAMP(7);
  } else {
    INDICES();
    constexpr float C2 = 0.088388347648318440f * 1.4426950408889634f;
    const int nt = NTILES;
    const int t0 = nt * ig + (ig < 4 ? 0 : ig < 12 ? 4 * (T_P1 - T_P2) : ig < 16 ? 4 * T_P1 + 8 * T_P2 - 12 * T_NU : ig < 24 ? 4 * T_P1 + 8 * T_P2 + 4 * T_NU - 16 * T_PL
                              : ig < 28 ? 2048 - 4 * T_C2 - 28 * T_C3 : 2048 - 32 * T_C2);
    const __amdgpu_buffer_rsrc_t rk = mkrs(kc + ((size_t)xg * SK + (size_t)t0 * 32) * DH, nt * 8192);
    const __amdgpu_buffer_rsrc_t rv = mkrs(vc + ((size_t)xg * SK + (size_t)t0 * 32) * DH, nt * 8192);
    int koff = (4 * (n & 3) + ((n >> 2) & 1)) * 256 + (n >> 3) * 64 + 16 * g, voff = lane * 16;
    v4u kf[3][8], vr[3][8];
    int id = w - 1;
#define ISSUE(d, dist) { \
      for (unsigned spins = 0; (int)__builtin_amdgcn_readfirstlane(ctl[2]) + (dist) <= id && spins < (1u << 22); ++spins) __builtin_amdgcn_s_sleep(1); \
      const int so_ = id * 8192; asm volatile("" : "+v"(koff), "+v"(voff) :: "memory"); \
      _Pragma("unroll") for (int u = 0; u < 8; ++u) kf[d][u] = ldnt(rk, koff + ((u >> 1) & 1) * 512 + (u & 1) * 128, so_ + (u >> 2) * 4096); \
      _Pragma("unroll") for (int u = 0; u < 8; ++u) vr[d][u] = ldnt(rv, voff + (u & 3) * 1024, so_ + (u >> 2) * 4096); \
      SCHED(); }
#define SIGNAL() { \
      SCHED(); \
      asm volatile("s_waitcnt vmcnt(%0)" ::"i"(SIGK) : "memory"); \
      if (lane == 0) __hip_atomic_fetch_add((LAS unsigned*)(L + L_CTL + 8), 1u, __ATOMIC_RELAXED, __HIP_MEMORY_SCOPE_WORKGROUP); \
      STAMP(32 + id); \
      SCHED(); \
      id += 7; }
#define QK(d) { \
      f32x4 sq[4]; \
      _Pragma("unroll") for (int t = 0; t < 4; ++t) sq[t] = mfma(kf[d][2 * t + 1], qf[1], mfma(kf[d][2 * t], qf[0], f32x4{0.f, 0.f, 0.f, 0.f})); \
      float sc[2][4]; \
      _Pragma("unroll") for (int j = 0; j < 8; ++j) { \
        auto x_ = __builtin_amdgcn_permlane32_swap(__float_as_uint(sq[2 * (j >> 2)][j & 3]), __float_as_uint(sq[2 * (j >> 2) + 1][j & 3]), false, false); \
        sc[j >> 2][j & 3] = __uint_as_float(x_[0]) + dpp<ROR4>(__uint_as_float(x_[1])); } \
      float tm = fmaxf(fmaxf(fmaxf(sc[0][0], sc[0][1]), fmaxf(sc[0][2], sc[0][3])), fmaxf(fmaxf(sc[1][0], sc[1][1]), fmaxf(sc[1][2], sc[1][3]))); \
      tm = max32(max16(tm)); \
      const float mn = fmaxf(m, tm * C2); \
      const float alpha = ex2(m - mn); \
      m = mn; \
      float p[8]; \
      _Pragma("unroll") for (int j = 0; j < 8; ++j) p[j] = ex2(sc[j >> 2][j & 3] * C2 - mn); \
      l = l * alpha + (((p[0] + p[1]) + (p[2] + p[3])) + ((p[4] + p[5]) + (p[6] + p[7]))); \
      pf = v4u{cvtpk(p[0], p[1]), cvtpk(p[2], p[3]), cvtpk(p[4], p[5]), cvtpk(p[6], p[7])}; \
      _Pragma("unroll") for (int e = 0; e < 8; ++e) acc[e] *= alpha; \
      asm volatile("" : "+v"(pf), "+v"(l), "+v"(acc[0]), "+v"(acc[1]), "+v"(acc[2]), "+v"(acc[3]), "+v"(acc[4]), "+v"(acc[5]), "+v"(acc[6]), "+v"(acc[7])); }
#define PV(d) { \
      _Pragma("unroll") for (int e = 0; e < 8; ++e) { \
        v4u bf; \
        _Pragma("unroll") for (int jj = 0; jj < 4; ++jj) \
          bf[jj] = __builtin_amdgcn_perm(vr[d][2 * jj + 1][e >> 1], vr[d][2 * jj][e >> 1], (e & 1) ? PHI : PLO); \
        acc[e] = mfma(bf, pf, acc[e]); } \
      SCHED(); }
    ISSUE(2, DIST0) SIGNAL() ISSUE(1, DIST0) SIGNAL() ISSUE(0, DIST0) SIGNAL()
    for (unsigned spins = 0; __builtin_amdgcn_readfirstlane(ctl[0]) == 0u && spins < (1u << 22); ++spins) __builtin_amdgcn_s_sleep(1);
    CBAR();
    v4u qf[2], pf;
#pragma unroll
    for (int s = 0; s < 2; ++s) qf[s] = *(const LAS v4u*)(L + L_Q + (n & 3) * 256 + 128 * s + 64 * ((n >> 2) & 1) + 16 * g);
    f32x4 acc[8];
#pragma unroll
    for (int e = 0; e < 8; ++e) acc[e] = f32x4{0.f, 0.f, 0.f, 0.f};
    float m = -1e30f, l = 0.f;
    if (w == 1) STAMP(8);
    QK(2) PV(2) QK(1) PV(1) QK(0)
#pragma unroll 1
    for (;;) {
      PV(0)
      if (id >= nt) break;
      ISSUE(0, (id >= nt - KEND ? DISTE : DIST))
      SIGNAL()
      QK(0)
    }
    l = sum32(sum16(l));
    if (n < 4) {
#pragma unroll
      for (int e = 0; e < 8; ++e) *(LAS f32x4*)(L + L_MO + ((w * 4 + n) * 128 + 32 * g + 4 * e) * 4) = acc[e];
      ((LAS float*)(L + L_MM))[n * 8 + w] = m; ((LAS float*)(L + L_ML))[n * 8 + w] = l;
    }
  }
  int pass = (DRY && w_0 == 0) ? 0 : 1;
  for (;;) {
    pass = __builtin_amdgcn_readfirstlane(pass);
    asm volatile("" : "+s"(pass));
#define PBAR() do { LGKM0(); if (pass) __builtin_amdgcn_s_barrier(); CBAR(); } while (0)
    INDICES();
    const unsigned tag = ctl[1];
    {
      const int r = w >> 1, sub = lane & 3;
      int a_m = L_MM + r * 32 + sub * 8, a_o = L_MO + (((2 * sub) * 4 + r) * 128 + (w & 1) * 64 + 4 * (lane >> 2)) * 4,
          sv = sub == 0 ? AO_OFF + (lane >> 2) * 8 : lane < 3 ? AML_OFF + (lane - 1) * 8 : OOB;
      asm volatile("" : "+v"(a_m), "+v"(a_o), "+v"(sv));
      PBAR();
      if (w == 0 && pass) STAMP(10);
      const f32x2 mo = *(const LAS f32x2*)(L + a_m), lo = *(const LAS f32x2*)(L + a_m + 128);
      const f32x4 oa = *(const LAS f32x4*)(L + a_o), ob = *(const LAS f32x4*)(L + a_o + 2048);
      float M = fmaxf(mo.x, mo.y);
      M = fmaxf(M, dpp<XOR1>(M)); M = fmaxf(M, dpp<XOR2>(M));
      const float wa = ex2(mo.x - M), wb = ex2(mo.y - M);
      const float Ls = sum_q(wa * lo.x + wb * lo.y);
      f32x4 o = wa * oa + wb * ob;
#pragma unroll
      for (int e = 0; e < 4; ++e) o[e] = sum_q(o[e]);
      v2u pk = sub == 0 ? v2u{cvtpk(o[0], o[1]), cvtpk(o[2], o[3])} : v2u{__float_as_uint(sub == 1 ? M : Ls), tag};
      asm volatile("" : "+v"(pk));
      if (pass) {
        __builtin_amdgcn_raw_buffer_store_b64(pk, rws, sv, (xg * 32 + ig) * 1024 + w * 128, AUX_SC1);
        if (w == 0) STAMP(11);
      }
    }
    if (isC2) {
      v4u wf[16];
#pragma unroll
      for (int k = 0; k < 16; ++k) wf[k] = *(const LAS v4u*)(L + L_W + ((w * 16 + k) * 64 + lane) * 16);
      const int sg = lane >> 2, dc = lane & 3;
      const int base = xg * 32768 + (hp & 1) * 512;
      int aw = L_ACT + (w * 32 + 8 * g + 2 * dc) * 2, ar = L_ACT + g * 16, sv = n < 2 ? n * 64 + g * 16 : OOB;
      asm volatile("" : "+v"(aw), "+v"(ar), "+v"(sv));
      v4u o4[2] = {}, ml[2] = {};
      for (unsigned spins = 0; pass; ++spins) {
        CBAR();
#pragma unroll
        for (int i = 0; i < 2; ++i) ml[i] = ldsc(rws, sg * 1024, AML_OFF + base + i * 16384 + (w >> 1) * 128);
#pragma unroll
        for (int i = 0; i < 2; ++i) o4[i] = ldsc(rws, sg * 1024 + dc * 16, AO_OFF + base + i * 16384 + w * 64);
        const bool bad = umax(umax4(o4[0]), umax4(o4[1])) == SENT || (((ml[0].y ^ tag) | (ml[0].w ^ tag)) | ((ml[1].y ^ tag) | (ml[1].w ^ tag))) != 0u;
        if (!__any(bad) || spins > SPIN_CAP) break;
      }
      if (w == 0 && pass) STAMP(12);
      float M = fmaxf(__uint_as_float(ml[0].x), __uint_as_float(ml[1].x));
      M = fmaxf(M, dpp<ROR4>(M)); M = fmaxf(M, dpp<ROR8>(M)); M = max32(max16(M));
      const float wa = ex2(__uint_as_float(ml[0].x) - M), wb = ex2(__uint_as_float(ml[1].x) - M);
      const float inv = rcp(sum32(sum16(sum_b23(wa * __uint_as_float(ml[0].z) + wb * __uint_as_float(ml[1].z)))));
      float o[8];
#pragma unroll
      for (int d = 0; d < 4; ++d) { o[2 * d] = wa * lo16(o4[0][d]) + wb * lo16(o4[1][d]); o[2 * d + 1] = wa * hi16(o4[0][d]) + wb * hi16(o4[1][d]); }
      *(LAS unsigned*)(L + aw) = cvtpk(sum_b23(row_tr(o[0], o[1], o[2], o[3])) * inv, sum_b23(row_tr(o[4], o[5], o[6], o[7])) * inv);
      PBAR();
      f32x4 y[4] = {};
#pragma unroll
      for (int k = 0; k < 8; ++k) {
        const v4u a = *(const LAS v4u*)(L + ar + k * 64);
        y[k & 1] = mfma(wf[k], a, y[k & 1]);
        y[2 + (k & 1)] = mfma(wf[8 + k], a, y[2 + (k & 1)]);
      }
      f32x4 ys = (n & 1) ? y[2] + y[3] : y[0] + y[1];
      asm volatile("" : "+v"(ys));
      if (pass) {
        stsc(asu(ys), rws, sv, BY_OFF + hp * 2048 + cq * 1024 + w * 128);
        if (w == 0) STAMP(13);
      }
    } else if (isC3) {
      v4u f1[8], f2[8];
#pragma unroll
      for (int k = 0; k < 8; ++k) f1[k] = *(const LAS v4u*)(L + L_W + ((w * 8 + k) * 64 + lane) * 16);
#pragma unroll
      for (int k = 0; k < 8; ++k) f2[k] = *(const LAS v4u*)(L + L_W + 65536 + ((w * 8 + k) * 64 + lane) * 16);
      const int col = 64 * w + 4 * n + g;
      const LAS u16* md = (const LAS u16*)(L + L_MOD);
      float ga = lo16(md[col]), sb = lo16(md[512 + col]), cb = rbf(1.0f + lo16(md[1024 + col])), xv = lo16(md[2048 + col]);
      int a_x1 = L_X1 + col * 4, a_st = L_ST + w * 8 + (lane >> 5) * 4, a_hw = L_ACT + col * 2, a_hr = L_ACT + w * 128 + g * 16, a_rw = L_RED + w * 256 + (n & 3) * 64 + g * 16,
          a_rr = L_RED + lane * 4, a_1w = L_H1 + w * 128 + lane * 2, a_1r = L_H1 + w * 128 + g * 16, sv = n < 4 ? n * 64 + g * 16 : OOB;
      asm volatile("" : "+v"(ga), "+v"(sb), "+v"(cb), "+v"(xv), "+v"(a_x1), "+v"(a_st), "+v"(a_hw), "+v"(a_hr), "+v"(a_rw), "+v"(a_rr), "+v"(a_1w), "+v"(a_1r), "+v"(sv));
      v4u yp[4] = {};
      for (unsigned spins = 0; pass; ++spins) {
        CBAR();
#pragma unroll
        for (int i = 0; i < 4; ++i) yp[i] = ldsc(rws, g * 2048 + n * 16, BY_OFF + i * 8192 + w * 256);
        if (!__any(umax(umax(umax4(yp[0]), umax4(yp[1])), umax(umax4(yp[2]), umax4(yp[3]))) == SENT) || spins > SPIN_CAP) break;
      }
      if (w == 0 && pass) STAMP(14);
      const f32x4 ys = (asf(yp[0]) + asf(yp[1])) + (asf(yp[2]) + asf(yp[3]));
      const float x1 = rbf(xv + rbf(ga * rbf(row_tr(ys[0], ys[1], ys[2], ys[3]))));
      *(LAS float*)(L + a_x1) = x1;
      *(LAS float*)(L + a_st) = sum_row(sum16(tr32(x1, x1 * x1)));
      PBAR();
      const f32x4 z = {0.f, 0.f, 0.f, 0.f};
      {
        const f32x4 t = (*(const LAS f32x4*)(L + L_ST) + *(const LAS f32x4*)(L + L_ST + 16)) + (*(const LAS f32x4*)(L + L_ST + 32) + *(const LAS f32x4*)(L + L_ST + 48));
        const float mean = (t[0] + t[2]) * (1.0f / D);
        const float var = rbf(fmaxf((t[1] + t[3]) * (1.0f / D) - mean * mean, 0.f));
        const float isd = rcp(rbf(__builtin_amdgcn_sqrtf(rbf(var + 1e-5f))));
        const float h = rbf(rbf(rbf(x1 - rbf(mean)) * isd) * cb) + sb;
        *(LAS u16*)(L + a_hw) = (u16)cvtpk(h, h);
        CBAR();
        const v4u a0 = *(const LAS v4u*)(L + a_hr), a1 = *(const LAS v4u*)(L + a_hr + 64);
        f32x4 t4[4];
#pragma unroll
        for (int k = 0; k < 4; ++k) t4[k] = mfma(f1[4 + k], a1, mfma(f1[k], a0, z));
        *(LAS f32x4*)(L + a_rw) = (n & 2) ? ((n & 1) ? t4[3] : t4[2]) : ((n & 1) ? t4[1] : t4[0]);
      }
      PBAR();
      {
        const LAS float* rr = (const LAS float*)(L + a_rr);
        const float hv = silu(rbf(((rr[0] + rr[64]) + (rr[128] + rr[192])) + ((rr[256] + rr[320]) + (rr[384] + rr[448]))));
        *(LAS u16*)(L + a_1w) = (u16)cvtpk(hv, hv);
        CBAR();
        const v4u a0 = *(const LAS v4u*)(L + a_1r), a1 = *(const LAS v4u*)(L + a_1r + 64);
        f32x4 t4[4];
#pragma unroll
        for (int k = 0; k < 4; ++k) t4[k] = mfma(f2[4 + k], a1, mfma(f2[k], a0, z));
        f32x4 ts = (n & 2) ? ((n & 1) ? t4[3] : t4[2]) : ((n & 1) ? t4[1] : t4[0]);
        asm volatile("" : "+v"(ts));
        if (pass) {
          stsc(asu(ts), rws, sv, CO_OFF + j3 * 2048 + w * 256);
          if (w == 0) STAMP(15);
        }
      }
      if (j3 < 16 && w == 0) {
        const int oc = 32 * j3 + 4 * (lane & 7) + g;
        const float xr = ((const LAS float*)(L + L_X1))[oc], gr = lo16(md[1536 + oc]);
        v4u cp[4] = {};
        for (unsigned spins = 0; pass; ++spins) {
          CBAR();
#pragma unroll
          for (int i = 0; i < 4; ++i) cp[i] = ldsc(rws, (lane >> 3) * 2048 + (lane & 7) * 16, CO_OFF + i * 16384 + j3 * 128);
          if (!__any(umax(umax(umax4(cp[0]), umax4(cp[1])), umax(umax4(cp[2]), umax4(cp[3]))) == SENT) || spins > SPIN_CAP) break;
        }
        if (pass) STAMP(16);
        const f32x4 cs = (asf(cp[0]) + asf(cp[1])) + (asf(cp[2]) + asf(cp[3]));
        float os = row_tr(cs[0], cs[1], cs[2], cs[3]);
        os += dpp<ROR8>(os);
        const float o = xr + rbf(gr * rbf(os));
        unsigned ob = cvtpk(o, o);
        asm volatile("" : "+v"(ob));
        if (pass) {
          if (n < 8) ((GAS u16*)out)[oc] = (u16)ob;
          if (j3 == 0 && lane == 0) __hip_atomic_store((gu32*)(ws + EPOCH_OFF), tag, RLX_AGENT);
          STAMP(17);
        }
      }
    }
    if (pass) break;
    pass = 1;
    STAMP(18);
  }
}

extern "C" void kernel_launch(void* const* d_in, const int* in_sizes, int n_in, void* d_out, int out_size, void* d_ws, size_t ws_size,
                              hipStream_t stream) {
  (void)in_sizes; (void)n_in; (void)out_size; (void)ws_size;
  (void)hipFuncSetAttribute((const void*)fused_decode, hipFuncAttributeMaxDynamicSharedMemorySize, L_BYTES);
  fused_decode<<<NWG, NTHR, L_BYTES, stream>>>((const u16*)d_in[0], (const u16*)d_in[1], (const u16*)d_in[2], (const u16*)d_in[3], (const u16*)d_in[4],
                                               (const u16*)d_in[6], (const u16*)d_in[7], (const u16*)d_in[8], (const u16*)d_in[9], (u16*)d_out,
                                               (unsigned char*)d_ws);
}
```

```cpp
#include <hip/hip_runtime.h>
#include <stdint.h>

#define GAS __attribute__((address_space(1)))
#define LAS __attribute__((address_space(3)))
typedef unsigned v4u __attribute__((ext_vector_type(4)));
typedef unsigned v2u __attribute__((ext_vector_type(2)));
typedef float f32x4 __attribute__((ext_vector_type(4)));
typedef float f32x2 __attribute__((ext_vector_type(2)));
typedef short bf16x8 __attribute__((ext_vector_type(8)));
typedef __bf16 bf16x2 __attribute__((ext_vector_type(2)));
typedef unsigned short u16;
typedef unsigned long long u64;
typedef GAS unsigned gu32;
typedef GAS u64 gu64;
#define RLX_AGENT __ATOMIC_RELAXED, __HIP_MEMORY_SCOPE_AGENT

#define DIST0 2
#define DIST 2
#define DISTE 3
#define KEND 6
#define SIGK 8
#define SLP1 4
#define SLP2 8
#define E1 1
#define E2 0
#define E3 1
#define SIGPRE 1
#define PACE 1
#define C2LEAD 3
#define PBIAS 0.25f
#define PPRE 1
#define PBIASN 1.0f
#define PGAINN 1.5f
#define PROUNDS 4
#define PT0 1500
#define PTS 600
#define PTD 150
#define PFIRST 22
#define PSTEP 8
#define PLAST 6
#define PTAU 57
#define NPEER 32
#define PGAIN 4.0f
#define CODEPF 154
#define WPRE 44
#define MWAIT 520
#define QWAIT 880
#define SLPA 4
#define SLPB 4
#define TLBW 0
#define TLBN 2
#define T_G 64
#define T_PL 68
#define T_C3 60
#define T_C2 60
static_assert(16 * T_G + 8 * T_PL + 4 * T_C3 + 4 * T_C2 <= 2048, "tiles of one kv head");
constexpr int T_X = 2048 - (16 * T_G + 8 * T_PL + 4 * T_C3 + 4 * T_C2);
static_assert(T_X <= 8, "");

constexpr int D = 512, SK = 65536, DH = 128, FF = 1792;
constexpr int NWG = 256, NW = 8, NTHR = NW * 64;
constexpr int AUX_NT = 2, AUX_SC1 = 16;
constexpr unsigned SENT = 0xFFFFFFFFu;
constexpr unsigned SPIN_CAP = 1u << 16;
constexpr int OOB = 0x40000000;
constexpr int EPOCH_OFF = 0;
constexpr int PRG_OFF = 32768;
constexpr int MODG_OFF = 4096;
constexpr int QG_OFF = 16384;
constexpr int AO_OFF = 65536;
constexpr int AML_OFF = AO_OFF + 256 * 1024;
constexpr int BY_OFF = AML_OFF + 256 * 1024;
constexpr int CO_OFF = BY_OFF + 32 * 1024;
constexpr int WS_BYTES = CO_OFF + 28 * 2048;
constexpr int STAMP_OFF = 16 << 20;
constexpr int L_W = 0;
constexpr int L_MO = 131072;
constexpr int L_MM = L_MO + 16384;
constexpr int L_ML = L_MM + 128;
constexpr int L_Q = L_ML + 128;
constexpr int L_ACT = L_Q + 1024;
constexpr int L_RED = L_ACT + 1024;
constexpr int L_ST = L_RED + 4096;
constexpr int L_H1 = L_ST + 64;
constexpr int L_X1 = L_H1 + 1024;
constexpr int L_MOD = L_X1 + 2048;
constexpr int L_CTL = L_MOD + 5120;
constexpr int L_STAMP = L_CTL + 32;
constexpr int L_BYTES = L_STAMP;
static_assert(L_BYTES <= 160 * 1024, "LDS");

__device__ __forceinline__ float lo16(unsigned w) { return __builtin_bit_cast(float, w << 16); }
__device__ __forceinline__ float hi16(unsigned w) { return __builtin_bit_cast(float, w & 0xffff0000u); }
__device__ __forceinline__ unsigned cvtpk(float lo, float hi) {
  bf16x2 r; r.x = (__bf16)lo; r.y = (__bf16)hi;
  return __builtin_bit_cast(unsigned, r);
}
#define rbf(x) __builtin_bit_cast(float, cvtpk(zero_, (x)))
__device__ __forceinline__ f32x4 mfma(v4u a, v4u b, f32x4 c) {
  return __builtin_amdgcn_mfma_f32_16x16x32_bf16(__builtin_bit_cast(bf16x8, a), __builtin_bit_cast(bf16x8, b), c, 0, 0, 0);
}
__device__ __forceinline__ float dot2(unsigned a, unsigned b, float acc) {
  return __builtin_amdgcn_fdot2_f32_bf16(__builtin_bit_cast(bf16x2, a), __builtin_bit_cast(bf16x2, b), acc, false);
}
__device__ __forceinline__ float rcp(float x) { return __builtin_amdgcn_rcpf(x); }
__device__ __forceinline__ float ex2(float x) { return __builtin_amdgcn_exp2f(x); }
__device__ __forceinline__ float silu(float x) { return x * rcp(1.0f + ex2(x * -1.4426950408889634f)); }
template <int CTRL> __device__ __forceinline__ float dpp(float x) {
  return __builtin_bit_cast(float, __builtin_amdgcn_mov_dpp(__builtin_bit_cast(int, x), CTRL, 0xf, 0xf, true));
}
constexpr int XOR1 = 0xB1, XOR2 = 0x4E, HMIR = 0x141, ROR4 = 0x124, ROR8 = 0x128;
__device__ __forceinline__ float sum16(float x) {
  auto s = __builtin_amdgcn_permlane16_swap(__float_as_uint(x), __float_as_uint(x), false, false);
  return __uint_as_float(s[0]) + __uint_as_float(s[1]);
}
__device__ __forceinline__ float sum32(float x) {
  auto s = __builtin_amdgcn_permlane32_swap(__float_as_uint(x), __float_as_uint(x), false, false);
  return __uint_as_float(s[0]) + __uint_as_float(s[1]);
}
__device__ __forceinline__ float max16(float x) {
  auto s = __builtin_amdgcn_permlane16_swap(__float_as_uint(x), __float_as_uint(x), false, false);
  return fmaxf(__uint_as_float(s[0]), __uint_as_float(s[1]));
}
__device__ __forceinline__ float max32(float x) {
  auto s = __builtin_amdgcn_permlane32_swap(__float_as_uint(x), __float_as_uint(x), false, false);
  return fmaxf(__uint_as_float(s[0]), __uint_as_float(s[1]));
}
__device__ __forceinline__ float sum_q(float x) { x += dpp<XOR1>(x); x += dpp<XOR2>(x); return x; }
__device__ __forceinline__ float sum_8(float x) { x = sum_q(x); x += dpp<HMIR>(x); return x; }
__device__ __forceinline__ float sum_b23(float x) { x += dpp<ROR4>(x); x += dpp<ROR8>(x); return x; }
__device__ __forceinline__ float sum_row(float x) { return sum_b23(sum_q(x)); }
__device__ __forceinline__ float wave_sum(float x) { return sum32(sum16(sum_row(x))); }
__device__ __forceinline__ float quad_tr(f32x4 v, int lane) {
  const bool b0 = lane & 1, b1 = lane & 2;
  const float t0 = (b0 ? v[1] : v[0]) + dpp<XOR1>(b0 ? v[0] : v[1]);
  const float t1 = (b0 ? v[3] : v[2]) + dpp<XOR1>(b0 ? v[2] : v[3]);
  return (b1 ? t1 : t0) + dpp<XOR2>(b1 ? t0 : t1);
}
__device__ __forceinline__ float tr16(float a, float b) {
  auto s = __builtin_amdgcn_permlane16_swap(__float_as_uint(a), __float_as_uint(b), false, false);
  return __uint_as_float(s[0]) + __uint_as_float(s[1]);
}
__device__ __forceinline__ float tr32(float a, float b) {
  auto s = __builtin_amdgcn_permlane32_swap(__float_as_uint(a), __float_as_uint(b), false, false);
  return __uint_as_float(s[0]) + __uint_as_float(s[1]);
}
__device__ __forceinline__ float row_tr(float a, float b, float c, float d) { return tr32(tr16(a, b), tr16(c, d)); }
#define LGKM0() asm volatile("s_waitcnt lgkmcnt(0)" ::: "memory")
#define VM0() asm volatile("s_waitcnt vmcnt(0)" ::: "memory")
#define BAR() do { LGKM0(); __builtin_amdgcn_s_barrier(); asm volatile("" ::: "memory"); } while (0)
#define SCHED() __builtin_amdgcn_sched_barrier(0)
#define CBAR() asm volatile("" ::: "memory")
__device__ __forceinline__ unsigned umax(unsigned a, unsigned b) { return a > b ? a : b; }
__device__ __forceinline__ unsigned umax4(v4u v) { return umax(umax(v.x, v.y), umax(v.z, v.w)); }
__device__ __forceinline__ __amdgpu_buffer_rsrc_t mkrs(const void* p, int bytes) { return __builtin_amdgcn_make_buffer_rsrc((void*)p, 0, bytes, 0x00020000); }
__device__ __forceinline__ v4u ldnt(__amdgpu_buffer_rsrc_t r, int vo, int so) { return __builtin_amdgcn_raw_buffer_load_b128(r, vo, so, AUX_NT); }
__device__ __forceinline__ v4u ldsc(__amdgpu_buffer_rsrc_t r, int vo, int so) { return __builtin_amdgcn_raw_buffer_load_b128(r, vo, so, AUX_SC1); }
__device__ __forceinline__ void stsc(v4u v, __amdgpu_buffer_rsrc_t r, int vo, int so) { __builtin_amdgcn_raw_buffer_store_b128(v, r, vo, so, AUX_SC1); }
__device__ __forceinline__ f32x4 asf(v4u v) { return __builtin_bit_cast(f32x4, v); }
__device__ __forceinline__ v4u asu(f32x4 v) { return __builtin_bit_cast(v4u, v); }
#define PLO 0x05040100u
#define PHI 0x07060302u

#define STAMP(k) do {} while (0)

__global__ void __launch_bounds__(NTHR, 2)
fused_decode(const u16* __restrict__ xin, const u16* __restrict__ kc, const u16* __restrict__ vc, const u16* __restrict__ wq,
             const u16* __restrict__ wo, const u16* __restrict__ w1, const u16* __restrict__ w2, const u16* __restrict__ cin,
             const u16* __restrict__ wmod, u16* __restrict__ out, unsigned char* ws) {
  extern __shared__ __attribute__((aligned(16))) unsigned char smem[];
  const u64 t_entry = __builtin_amdgcn_s_memrealtime();
  unsigned pf0 = 0, pf1 = 0;
  {
    const GAS unsigned char* pc = (const GAS unsigned char*)(__builtin_amdgcn_s_getpc() & ~127ull);
    if (threadIdx.x < CODEPF) pf0 = *(const GAS unsigned*)(pc + threadIdx.x * 128);
  }
  LAS unsigned char* L = (LAS unsigned char*)smem;
  volatile LAS unsigned* ctl = (volatile LAS unsigned*)(L + L_CTL);
  const int w_0 = __builtin_amdgcn_readfirstlane(threadIdx.x >> 6);
#define INDICES() \
  int lane = threadIdx.x & 63, w = w_0, b = blockIdx.x; \
  asm volatile("" : "+v"(lane)); asm volatile("" : "+s"(w)); asm volatile("" : "+s"(b)); \
  float zero_ = 0.f; asm volatile("" : "+v"(zero_)); \
  const int g = lane >> 4, n = lane & 15, xg = b & 7, ig = b >> 3; \
  const bool isC2 = ig >= 28, isC3 = ig >= 24 && ig < 28 && xg < 7; \
  const int hp = 2 * xg + ((ig >> 1) & 1), cq = ig & 1;         \
  const int j3 = 4 * xg + (ig - 24);                            \
  (void)g; (void)n; (void)isC2; (void)isC3; (void)hp; (void)cq; (void)j3;
  asm volatile("" ::"s"(xin), "s"(kc), "s"(vc), "s"(wq), "s"(wo), "s"(w1), "s"(w2), "s"(cin), "s"(wmod), "s"(out), "s"(ws));
  const __amdgpu_buffer_rsrc_t rws = mkrs(ws, WS_BYTES);
  if (threadIdx.x == 0) { ctl[0] = 0u; ctl[2] = 0u; ctl[3] = 0u; ctl[4] = 0u; }
  BAR();
  LAS float* red = (LAS float*)(L + L_RED);
#define NTILES (ig < 16 ? T_G : ig < 16 + T_X ? T_PL + 1 : ig < 24 ? T_PL : ig < 28 ? T_C3 : T_C2)
#define TILE0 (NTILES * ig + (ig < 16 ? 0 : ig < 16 + T_X ? 16 * (T_G - T_PL - 1) : ig < 24 ? 16 * (T_G - T_PL) + T_X : ig < 28 ? 2048 - 4 * T_C2 - 28 * T_C3 : 2048 - 32 * T_C2))
#define GCOL (ig < 8 ? 1024 + 256 * xg + 64 * (ig - 4) : 512 * xg + 64 * (ig - 8))
#define TAG() (__hip_atomic_load((gu32*)(ws + EPOCH_OFF), RLX_AGENT) + 1u)
  unsigned tag0 = 0; v4u in8 = {};
  {
    INDICES();
    if (w == 0) {
      STAMP(1);
      tag0 = __hip_atomic_load((gu32*)(ws + EPOCH_OFF), RLX_AGENT);
#pragma unroll
      for (int d = 0; d < 4; ++d) in8[d] = ((const GAS unsigned*)(ig < 8 ? cin : xin))[lane + 64 * d];
    }
    if (ig < 4) {
      const __amdgpu_buffer_rsrc_t rm = mkrs(wmod, D * 6144);
      const int c0 = 128 * xg + 32 * ig;
      v4u R[4];
#pragma unroll
      for (int j = 0; j < 4; ++j) R[j] = ldnt(rm, (4 * (lane >> 2)) * 6144 + (lane & 3) * 16, (64 * w + j) * 6144 + c0 * 2);
      const v2u c4 = *(const GAS v2u*)((const GAS u16*)cin + 64 * w + 4 * (lane >> 2));
      stsc(v4u{SENT, SENT, SENT, SENT}, rws, lane < 32 ? lane * 16 : OOB, BY_OFF + ((4 * xg + ig) * 8 + w) * 512);
      SCHED();
      const unsigned a0 = cvtpk(silu(lo16(c4.x)), silu(hi16(c4.x))), a1 = cvtpk(silu(lo16(c4.y)), silu(hi16(c4.y)));
      float acc[8];
#pragma unroll
      for (int d = 0; d < 4; ++d) {
        acc[2 * d] = dot2(__builtin_amdgcn_perm(R[3][d], R[2][d], PLO), a1, dot2(__builtin_amdgcn_perm(R[1][d], R[0][d], PLO), a0, 0.f));
        acc[2 * d + 1] = dot2(__builtin_amdgcn_perm(R[3][d], R[2][d], PHI), a1, dot2(__builtin_amdgcn_perm(R[1][d], R[0][d], PHI), a0, 0.f));
      }
      const float t0 = sum_b23(row_tr(acc[0], acc[2], acc[4], acc[6])), t1 = sum_b23(row_tr(acc[1], acc[3], acc[5], acc[7]));
      *(LAS f32x2*)(red + w * 32 + (n & 3) * 8 + g * 2) = f32x2{t0, t1};
      VM0();
      BAR();
      if (w == 0 && lane < 16) {
        f32x2 t = {0.f, 0.f};
#pragma unroll
        for (int i = 0; i < NW; ++i) t += *(const LAS f32x2*)(red + i * 32 + 2 * lane);
        __hip_atomic_store((gu64*)(ws + MODG_OFF) + (c0 >> 1) + lane, ((u64)(tag0 + 1u) << 32) | cvtpk(t.x, t.y), RLX_AGENT);
      }
      if (w == 0) STAMP(4);
    } else if (ig < 16 || isC2 || isC3) {
      const int ro = lane >> 3, c8 = lane & 7;
      const bool one = ig < 16;
      v4u R[2][8];
#pragma unroll
      for (int h = 0; h < 2; ++h) {
        const int rs = (isC2 || (isC3 && h)) ? D * 2 : isC3 ? FF * 2 : ig < 8 ? 6144 : 8192;
        const __amdgpu_buffer_rsrc_t r = mkrs(isC2 ? wo : isC3 ? (h ? w2 : w1) : ig < 8 ? wmod : wq, (one && h) ? 0 : isC2 ? 4096 * D * 2 : isC3 ? D * FF * 2 : D * rs);
        const int vo = isC2 ? (8 * g) * (D * 2) + n * 16 : (8 * ro) * rs + c8 * 16;
        const int so = isC2 ? (256 * hp + 32 * w) * (D * 2) + cq * 512 + h * 256 : (isC3 && h) ? 64 * j3 * (D * 2) + w * 128 : 64 * w * rs + (isC3 ? 64 * j3 : GCOL) * 2;
#pragma unroll
        for (int u = 0; u < 8; ++u) R[h][u] = ldnt(r, vo, so + u * rs);
      }
      stsc(v4u{SENT, SENT, SENT, SENT}, rws, (ig >= 8 && one && lane < 32) ? lane * 16 : OOB, AO_OFF + xg * 32768 + ((ig & 7) * 8 + w) * 512);
      SCHED();
      const int la = isC2 ? (((n >> 1) * 8 + w) * 64 + g * 16 + 8 * (n & 1)) * 16 : (((w * 2 + (ro >> 2)) * 4 + (c8 >> 1)) * 64 + (ro & 3) * 16 + 8 * (c8 & 1)) * 16;
#pragma unroll
      for (int h = 0; h < 2; ++h) {
        if (h && one) break;
#pragma unroll
        for (int e = 0; e < 8; ++e) {
          v4u u;
#pragma unroll
          for (int jj = 0; jj < 4; ++jj) u[jj] = __builtin_amdgcn_perm(R[h][2 * jj + 1][e >> 1], R[h][2 * jj][e >> 1], (e & 1) ? PHI : PLO);
          *(LAS v4u*)(L + L_W + h * 65536 + la + e * 16) = u;
        }
      }
      VM0();
      LGKM0();
      if (lane == 0) __hip_atomic_fetch_add((LAS unsigned*)(L + L_CTL + 16), 1u, __ATOMIC_RELAXED, __HIP_MEMORY_SCOPE_WORKGROUP);
      if (w == 0) STAMP(2);
    }
  }
  if (w_0 == 0) {
    INDICES();
    __builtin_amdgcn_s_setprio(3);
    const unsigned tag = tag0 + 1u;
    if (ig >= 4 && ig < 16) {
      const bool isq = ig >= 8;
      for (unsigned spins = 0; __builtin_amdgcn_readfirstlane(ctl[4]) < 8u && spins < (1u << 22); ++spins) __builtin_amdgcn_s_sleep(1);
      CBAR();
      v4u wf[WPRE];
#pragma unroll
      for (int k = 0; k < WPRE; ++k) { wf[k] = *(const LAS v4u*)(L + L_W + (k * 64 + lane) * 16); }
#pragma unroll
      for (int k = 0; k < WPRE; ++k) asm volatile("" : "+v"(wf[k]));
      float xv[8];
#pragma unroll
      for (int d = 0; d < 4; ++d) { xv[2 * d] = lo16(in8[d]); xv[2 * d + 1] = hi16(in8[d]); }
      if (isq) {
        const float mean = wave_sum(((xv[0] + xv[1]) + (xv[2] + xv[3])) + ((xv[4] + xv[5]) + (xv[6] + xv[7]))) * (1.0f / D);
        float sq = 0.f;
#pragma unroll
        for (int e = 0; e < 8; ++e) sq += (xv[e] - mean) * (xv[e] - mean);
        const float var = rbf(wave_sum(sq) * (1.0f / D));
        const float mb = rbf(mean), isd = rcp(rbf(__builtin_amdgcn_sqrtf(rbf(var + 1e-5f))));
#pragma unroll
        for (int e = 0; e < 8; ++e) xv[e] = rbf(rbf(xv[e] - mb) * isd);
        for (unsigned spins = 0; __builtin_amdgcn_s_memrealtime() - t_entry < MWAIT && spins < 1024; ++spins) __builtin_amdgcn_s_sleep(2);
        const gu64* gs = (const gu64*)(ws + MODG_OFF) + lane;
        u64 v[8];
        for (unsigned spins = 0;; ++spins) {
          bool ok = true;
#pragma unroll
          for (int d = 0; d < 8; ++d) v[d] = __hip_atomic_load((gu64*)gs + 64 * d, RLX_AGENT);
#pragma unroll
          for (int d = 0; d < 8; ++d) ok &= (unsigned)(v[d] >> 32) == tag;
          if (__all(ok) || spins > SPIN_CAP) break;
          __builtin_amdgcn_s_sleep(SLP1);
        }
        STAMP(3);
#pragma unroll
        for (int d = 0; d < 4; ++d) {
          const unsigned sa = (unsigned)v[d], ca = (unsigned)v[4 + d];
          xv[2 * d] = rbf(xv[2 * d] * rbf(1.0f + lo16(ca))) + lo16(sa);
          xv[2 * d + 1] = rbf(xv[2 * d + 1] * rbf(1.0f + hi16(ca))) + hi16(sa);
        }
      } else {
#pragma unroll
        for (int e = 0; e < 8; ++e) xv[e] = silu(xv[e]);
      }
#pragma unroll
      for (int d = 0; d < 4; ++d) ((LAS unsigned*)(L + L_ACT))[lane + 64 * d] = cvtpk(xv[2 * d], xv[2 * d + 1]);
      CBAR();
      f32x4 acc[4] = {};
#pragma unroll
      for (int k = 0; k < 16; ++k) {
        const v4u a = *(const LAS v4u*)(L + L_ACT + k * 64 + g * 16);
#pragma unroll
        for (int t = 0; t < 4; ++t) acc[t] = mfma(k * 4 + t < WPRE ? wf[(k * 4 + t) % WPRE] : *(const LAS v4u*)(L + L_W + ((k * 4 + t) * 64 + lane) * 16), a, acc[t]);
      }
      const f32x4 ts = (n & 4) ? ((n & 2) ? acc[3] : acc[2]) : ((n & 2) ? acc[1] : acc[0]);
      if (n < 8)
        __hip_atomic_store((gu64*)(ws + (isq ? QG_OFF : MODG_OFF)) + (GCOL >> 1) + 8 * (n >> 1) + 2 * g + (n & 1),
                           ((u64)tag << 32) | ((n & 1) ? cvtpk(ts[2], ts[3]) : cvtpk(ts[0], ts[1])), RLX_AGENT);
      if (isq) STAMP(5);
    }
    for (unsigned spins = 0; __builtin_amdgcn_s_memrealtime() - t_entry < QWAIT && spins < 1024; ++spins) __builtin_amdgcn_s_sleep(4);
    {
      const gu64* gs = (const gu64*)(ws + QG_OFF) + 256 * xg + lane;
      u64 v[4];
      for (unsigned spins = 0;; ++spins) {
        bool ok = true;
#pragma unroll
        for (int d = 0; d < 4; ++d) v[d] = __hip_atomic_load((gu64*)gs + 64 * d, RLX_AGENT);
#pragma unroll
        for (int d = 0; d < 4; ++d) ok &= (unsigned)(v[d] >> 32) == tag;
        if (__all(ok) || spins > SPIN_CAP) break;
        __builtin_amdgcn_s_sleep(SLP2);
      }
#pragma unroll
      for (int d = 0; d < 4; ++d) ((LAS unsigned*)(L + L_Q))[64 * d + lane] = (unsigned)v[d];
      ctl[1] = tag;
      LGKM0();
      ctl[0] = 1u;
      STAMP(6);
    }
    __builtin_amdgcn_s_setprio(0);
    if (isC3) {
      const gu64* gm = (const gu64*)(ws + MODG_OFF) + 512 + lane;
      u64 t[16];
      for (unsigned spins = 0;; ++spins) {
        bool ok = true;
#pragma unroll
        for (int d = 0; d < 16; ++d) t[d] = __hip_atomic_load((gu64*)gm + 64 * d, RLX_AGENT);
#pragma unroll
        for (int d = 0; d < 16; ++d) ok &= (unsigned)(t[d] >> 32) == tag;
        if (__all(ok) || spins > SPIN_CAP) break;
      }
#pragma unroll
      for (int d = 0; d < 16; ++d) ((LAS unsigned*)(L + L_MOD))[64 * d + lane] = (unsigned)t[d];
#pragma unroll
      for (int d = 0; d < 4; ++d) ((LAS unsigned*)(L + L_MOD + 4096))[lane + 64 * d] = in8[d];
    }
    *(LAS f32x4*)(L + L_MO + lane * 32) = f32x4{0.f, 0.f, 0.f, 0.f};
    *(LAS f32x4*)(L + L_MO + lane * 32 + 16) = f32x4{0.f, 0.f, 0.f, 0.f};
    if (lane < 4) { ((LAS float*)(L + L_MM))[lane * 8] = -1e30f; ((LAS float*)(L + L_ML))[lane * 8] = 0.f; }
    STAMP(7);
    {
      const int nt = NTILES + (isC2 ? C2LEAD : 0);
#pragma unroll 1
      for (int rd = 0; rd < PROUNDS; ++rd) {
        const unsigned tp = PT0 + rd * PTS;
        for (unsigned spins = 0; __builtin_amdgcn_s_memrealtime() - t_entry < tp && spins < 4096; ++spins) __builtin_amdgcn_s_sleep(4);
        const int rem = nt - (int)__builtin_amdgcn_readfirstlane(ctl[2]);
        if (rem <= 6) break;
        if (lane == 0) __hip_atomic_store((gu64*)(ws + PRG_OFF) + rd * 256 + b, ((u64)tag << 32) | (unsigned)rem, RLX_AGENT);
        for (unsigned spins = 0; __builtin_amdgcn_s_memrealtime() - t_entry < tp + PTD && spins < 4096; ++spins) __builtin_amdgcn_s_sleep(4);
        if (PPRE) ctl[3] = ctl[2] + 6;
        CBAR();
        const v4u pa = ldsc(rws, lane * 16, PRG_OFF + rd * 2048), pb = ldsc(rws, lane * 16, PRG_OFF + rd * 2048 + 1024);
        const float cnt = wave_sum((pa.y == tag ? 1.f : 0.f) + (pa.w == tag ? 1.f : 0.f) + (pb.y == tag ? 1.f : 0.f) + (pb.w == tag ? 1.f : 0.f));
        const float sum = wave_sum((pa.y == tag ? (float)pa.x : 0.f) + (pa.w == tag ? (float)pa.z : 0.f) + (pb.y == tag ? (float)pb.x : 0.f) + (pb.w == tag ? (float)pb.z : 0.f));
        const float lag = (float)rem - sum * rcp(fmaxf(cnt, 1.f));
        const int here = (int)ctl[2] + 2;
        ctl[3] = (unsigned)(lag > PBIAS ? here + (int)(PGAIN * (lag - PBIAS)) : lag < -PBIASN ? -(here + (int)(PGAINN * (-lag - PBIASN))) : 0);
      }
    }
  } else {
    INDICES();
    constexpr float C2 = 0.088388347648318440f * 1.4426950408889634f;
    const int nt = NTILES;
    const int t0 = TILE0;
    const __amdgpu_buffer_rsrc_t rk = mkrs(kc + ((size_t)xg * SK + (size_t)t0 * 32) * DH, nt * 8192);
    const __amdgpu_buffer_rsrc_t rv = mkrs(vc + ((size_t)xg * SK + (size_t)t0 * 32) * DH, nt * 8192);
    int koff = (4 * (n & 3) + ((n >> 2) & 1)) * 256 + (n >> 3) * 64 + 16 * g, voff = lane * 16;
    v4u kf[3][8], vr[3][8];
    int id = w - 1;
#define ISSUE(d, dist) { \
      for (unsigned spins = 0; spins < (1u << 22); ++spins) { \
        const u64 c_ = *(volatile LAS u64*)(L + L_CTL + 8); \
        const int u_ = (int)__builtin_amdgcn_readfirstlane((unsigned)(c_ >> 32));        \
        if ((int)__builtin_amdgcn_readfirstlane((unsigned)c_) + (dist) + (id < u_ ? 1 : id < -u_ ? -1 : 0) > id) break; \
        __builtin_amdgcn_s_sleep(1); } \
      const int so_ = id * 8192; asm volatile("" : "+v"(koff), "+v"(voff) :: "memory"); \
      _Pragma("unroll") for (int u = 0; u < 8; ++u) kf[d][u] = ldnt(rk, koff + ((u >> 1) & 1) * 512 + (u & 1) * 128, so_ + (u >> 2) * 4096); \
      _Pragma("unroll") for (int u = 0; u < 8; ++u) vr[d][u] = ldnt(rv, voff + (u & 3) * 1024, so_ + (u >> 2) * 4096); \
      SCHED(); }
#define SIGNAL() { \
      SCHED(); \
      asm volatile("s_waitcnt vmcnt(%0)" ::"i"(SIGK) : "memory"); \
      if (lane == 0) __hip_atomic_fetch_add((LAS unsigned*)(L + L_CTL + 8), 1u, __ATOMIC_RELAXED, __HIP_MEMORY_SCOPE_WORKGROUP); \
      STAMP(32 + id); \
      SCHED(); \
      id += 7; }
#define QK(d) { \
      f32x4 sq[4]; \
      _Pragma("unroll") for (int t = 0; t < 4; ++t) sq[t] = mfma(kf[d][2 * t + 1], qf[1], mfma(kf[d][2 * t], qf[0], f32x4{0.f, 0.f, 0.f, 0.f})); \
      float sc[2][4]; \
      _Pragma("unroll") for (int j = 0; j < 8; ++j) { \
        auto x_ = __builtin_amdgcn_permlane32_swap(__float_as_uint(sq[2 * (j >> 2)][j & 3]), __float_as_uint(sq[2 * (j >> 2) + 1][j & 3]), false, false); \
        sc[j >> 2][j & 3] = __uint_as_float(x_[0]) + dpp<ROR4>(__uint_as_float(x_[1])); } \
      float tm = fmaxf(fmaxf(fmaxf(sc[0][0], sc[0][1]), fmaxf(sc[0][2], sc[0][3])), fmaxf(fmaxf(sc[1][0], sc[1][1]), fmaxf(sc[1][2], sc[1][3]))); \
      tm = max32(max16(tm)); \
      const float mn = fmaxf(m, tm * C2); \
      const float alpha = ex2(m - mn); \
      m = mn; \
      float p[8]; \
      _Pragma("unroll") for (int j = 0; j < 8; ++j) p[j] = ex2(sc[j >> 2][j & 3] * C2 - mn); \
      l = l * alpha + (((p[0] + p[1]) + (p[2] + p[3])) + ((p[4] + p[5]) + (p[6] + p[7]))); \
      pf = v4u{cvtpk(p[0], p[1]), cvtpk(p[2], p[3]), cvtpk(p[4], p[5]), cvtpk(p[6], p[7])}; \
      _Pragma("unroll") for (int e = 0; e < 8; ++e) acc[e] *= alpha; \
      asm volatile("" : "+v"(pf), "+v"(l), "+v"(acc[0]), "+v"(acc[1]), "+v"(acc[2]), "+v"(acc[3]), "+v"(acc[4]), "+v"(acc[5]), "+v"(acc[6]), "+v"(acc[7])); }
#define PV(d) { \
      _Pragma("unroll") for (int e = 0; e < 8; ++e) { \
        v4u bf; \
        _Pragma("unroll") for (int jj = 0; jj < 4; ++jj) \
          bf[jj] = __builtin_amdgcn_perm(vr[d][2 * jj + 1][e >> 1], vr[d][2 * jj][e >> 1], (e & 1) ? PHI : PLO); \
        acc[e] = mfma(bf, pf, acc[e]); } \
      SCHED(); }
    ISSUE(2, DIST0) SIGNAL() ISSUE(1, DIST0) SIGNAL() ISSUE(0, DIST0) SIGNAL()
    for (unsigned spins = 0; __builtin_amdgcn_readfirstlane(ctl[0]) == 0u && spins < (1u << 22); ++spins) __builtin_amdgcn_s_sleep(1);
    CBAR();
    v4u qf[2], pf;
#pragma unroll
    for (int s = 0; s < 2; ++s) qf[s] = *(const LAS v4u*)(L + L_Q + (n & 3) * 256 + 128 * s + 64 * ((n >> 2) & 1) + 16 * g);
    f32x4 acc[8];
#pragma unroll
    for (int e = 0; e < 8; ++e) acc[e] = f32x4{0.f, 0.f, 0.f, 0.f};
    float m = -1e30f, l = 0.f;
    if (w == 1) STAMP(8);
    QK(2) PV(2) QK(1) PV(1) QK(0)
#pragma unroll 1
    for (;;) {
      PV(0)
      if (id >= nt) break;
      ISSUE(0, (id >= nt - KEND ? DISTE : DIST))
      SIGNAL()
      QK(0)
    }
    l = sum32(sum16(l));
    if (n < 4) {
#pragma unroll
      for (int e = 0; e < 8; ++e) *(LAS f32x4*)(L + L_MO + ((w * 4 + n) * 128 + 32 * g + 4 * e) * 4) = acc[e];
      ((LAS float*)(L + L_MM))[n * 8 + w] = m; ((LAS float*)(L + L_ML))[n * 8 + w] = l;
    }
  }
  asm volatile("" ::"v"(pf0), "v"(pf1));
  {
    constexpr bool pass = true;
#define PBAR() BAR()
    INDICES();
    const unsigned tag = ctl[1];
    {
      const int r = w >> 1, sub = lane & 3;
      int a_m = L_MM + r * 32 + sub * 8, a_o = L_MO + (((2 * sub) * 4 + r) * 128 + (w & 1) * 64 + 4 * (lane >> 2)) * 4,
          sv = sub == 0 ? AO_OFF + (lane >> 2) * 8 : lane < 3 ? AML_OFF + (lane - 1) * 8 : OOB;
      asm volatile("" : "+v"(a_m), "+v"(a_o), "+v"(sv));
      PBAR();
      if (w == 0 && pass) STAMP(10);
      const f32x2 mo = *(const LAS f32x2*)(L + a_m), lo = *(const LAS f32x2*)(L + a_m + 128);
      const f32x4 oa = *(const LAS f32x4*)(L + a_o), ob = *(const LAS f32x4*)(L + a_o + 2048);
      float M = fmaxf(mo.x, mo.y);
      M = fmaxf(M, dpp<XOR1>(M)); M = fmaxf(M, dpp<XOR2>(M));
      const float wa = ex2(mo.x - M), wb = ex2(mo.y - M);
      const float Ls = sum_q(wa * lo.x + wb * lo.y);
      f32x4 o = wa * oa + wb * ob;
#pragma unroll
      for (int e = 0; e < 4; ++e) o[e] = sum_q(o[e]);
      v2u pk = sub == 0 ? v2u{cvtpk(o[0], o[1]), cvtpk(o[2], o[3])} : v2u{__float_as_uint(sub == 1 ? M : Ls), tag};
      asm volatile("" : "+v"(pk));
      if (pass) {
        __builtin_amdgcn_raw_buffer_store_b64(pk, rws, sv, (xg * 32 + ig) * 1024 + w * 128, AUX_SC1);
        if (w == 0) STAMP(11);
      }
    }
    if (isC2) {
      v4u wf[16];
#pragma unroll
      for (int k = 0; k < 16; ++k) wf[k] = *(const LAS v4u*)(L + L_W + ((w * 16 + k) * 64 + lane) * 16);
      const int sg = lane >> 2, dc = lane & 3;
      const int base = xg * 32768 + (hp & 1) * 512;
      int aw = L_ACT + (w * 32 + 8 * g + 2 * dc) * 2, ar = L_ACT + g * 16, sv = n < 2 ? n * 64 + g * 16 : OOB;
      asm volatile("" : "+v"(aw), "+v"(ar), "+v"(sv));
      v4u o4[2] = {}, ml[2] = {};
      for (unsigned spins = 0; pass; ++spins) {
        CBAR();
#pragma unroll
        for (int i = 0; i < 2; ++i) ml[i] = ldsc(rws, sg * 1024, AML_OFF + base + i * 16384 + (w >> 1) * 128);
#pragma unroll
        for (int i = 0; i < 2; ++i) o4[i] = ldsc(rws, sg * 1024 + dc * 16, AO_OFF + base + i * 16384 + w * 64);
        const bool bad = umax(umax4(o4[0]), umax4(o4[1])) == SENT || (((ml[0].y ^ tag) | (ml[0].w ^ tag)) | ((ml[1].y ^ tag) | (ml[1].w ^ tag))) != 0u;
        if (!__any(bad) || spins > SPIN_CAP) break;
        if (SLPA) __builtin_amdgcn_s_sleep(SLPA);
      }
      if (w == 0 && pass) STAMP(12);
      float M = fmaxf(__uint_as_float(ml[0].x), __uint_as_float(ml[1].x));
      M = fmaxf(M, dpp<ROR4>(M)); M = fmaxf(M, dpp<ROR8>(M)); M = max32(max16(M));
      const float wa = ex2(__uint_as_float(ml[0].x) - M), wb = ex2(__uint_as_float(ml[1].x) - M);
      const float inv = rcp(sum32(sum16(sum_b23(wa * __uint_as_float(ml[0].z) + wb * __uint_as_float(ml[1].z)))));
      float o[8];
#pragma unroll
      for (int d = 0; d < 4; ++d) { o[2 * d] = wa * lo16(o4[0][d]) + wb * lo16(o4[1][d]); o[2 * d + 1] = wa * hi16(o4[0][d]) + wb * hi16(o4[1][d]); }
      *(LAS unsigned*)(L + aw) = cvtpk(sum_b23(row_tr(o[0], o[1], o[2], o[3])) * inv, sum_b23(row_tr(o[4], o[5], o[6], o[7])) * inv);
      PBAR();
      f32x4 y[4] = {};
#pragma unroll
      for (int k = 0; k < 8; ++k) {
        const v4u a = *(const LAS v4u*)(L + ar + k * 64);
        y[k & 1] = mfma(wf[k], a, y[k & 1]);
        y[2 + (k & 1)] = mfma(wf[8 + k], a, y[2 + (k & 1)]);
      }
      f32x4 ys = (n & 1) ? y[2] + y[3] : y[0] + y[1];
      asm volatile("" : "+v"(ys));
      if (pass) {
        stsc(asu(ys), rws, sv, BY_OFF + hp * 2048 + cq * 1024 + w * 128);
        if (w == 0) STAMP(13);
      }
    } else if (isC3) {
      v4u f1[8], f2[8];
#pragma unroll
      for (int k = 0; k < 8; ++k) f1[k] = *(const LAS v4u*)(L + L_W + ((w * 8 + k) * 64 + lane) * 16);
#pragma unroll
      for (int k = 0; k < 8; ++k) f2[k] = *(const LAS v4u*)(L + L_W + 65536 + ((w * 8 + k) * 64 + lane) * 16);
      const int col = 64 * w + 4 * n + g;
      const LAS u16* md = (const LAS u16*)(L + L_MOD);
      float ga = lo16(md[col]), sb = lo16(md[512 + col]), cb = rbf(1.0f + lo16(md[1024 + col])), xv = lo16(md[2048 + col]);
      int a_x1 = L_X1 + col * 4, a_st = L_ST + w * 8 + (lane >> 5) * 4, a_hw = L_ACT + col * 2, a_hr = L_ACT + w * 128 + g * 16, a_rw = L_RED + w * 256 + (n & 3) * 64 + g * 16,
          a_rr = L_RED + lane * 4, a_1w = L_H1 + w * 128 + lane * 2, a_1r = L_H1 + w * 128 + g * 16, sv = n < 4 ? n * 64 + g * 16 : OOB;
      asm volatile("" : "+v"(ga), "+v"(sb), "+v"(cb), "+v"(xv), "+v"(a_x1), "+v"(a_st), "+v"(a_hw), "+v"(a_hr), "+v"(a_rw), "+v"(a_rr), "+v"(a_1w), "+v"(a_1r), "+v"(sv));
      v4u yp[4] = {};
      for (unsigned spins = 0; pass; ++spins) {
        CBAR();
#pragma unroll
        for (int i = 0; i < 4; ++i) yp[i] = ldsc(rws, g * 2048 + n * 16, BY_OFF + i * 8192 + w * 256);
        if (!__any(umax(umax(umax4(yp[0]), umax4(yp[1])), umax(umax4(yp[2]), umax4(yp[3]))) == SENT) || spins > SPIN_CAP) break;
        if (SLPB) __builtin_amdgcn_s_sleep(SLPB);
      }
      if (w == 0 && pass) STAMP(14);
      const f32x4 ys = (asf(yp[0]) + asf(yp[1])) + (asf(yp[2]) + asf(yp[3]));
      const float x1 = rbf(xv + rbf(ga * rbf(row_tr(ys[0], ys[1], ys[2], ys[3]))));
      *(LAS float*)(L + a_x1) = x1;
      *(LAS float*)(L + a_st) = sum_row(sum16(tr32(x1, x1 * x1)));
      PBAR();
      const f32x4 z = {0.f, 0.f, 0.f, 0.f};
      {
        const f32x4 t = (*(const LAS f32x4*)(L + L_ST) + *(const LAS f32x4*)(L + L_ST + 16)) + (*(const LAS f32x4*)(L + L_ST + 32) + *(const LAS f32x4*)(L + L_ST + 48));
        const float mean = (t[0] + t[2]) * (1.0f / D);
        const float var = rbf(fmaxf((t[1] + t[3]) * (1.0f / D) - mean * mean, 0.f));
        const float isd = rcp(rbf(__builtin_amdgcn_sqrtf(rbf(var + 1e-5f))));
        const float h = rbf(rbf(rbf(x1 - rbf(mean)) * isd) * cb) + sb;
        *(LAS u16*)(L + a_hw) = (u16)cvtpk(h, h);
        CBAR();
        const v4u a0 = *(const LAS v4u*)(L + a_hr), a1 = *(const LAS v4u*)(L + a_hr + 64);
        f32x4 t4[4];
#pragma unroll
        for (int k = 0; k < 4; ++k) t4[k] = mfma(f1[4 + k], a1, mfma(f1[k], a0, z));
        *(LAS f32x4*)(L + a_rw) = (n & 2) ? ((n & 1) ? t4[3] : t4[2]) : ((n & 1) ? t4[1] : t4[0]);
      }
      PBAR();
      {
        const LAS float* rr = (const LAS float*)(L + a_rr);
        const float hv = silu(rbf(((rr[0] + rr[64]) + (rr[128] + rr[192])) + ((rr[256] + rr[320]) + (rr[384] + rr[448]))));
        *(LAS u16*)(L + a_1w) = (u16)cvtpk(hv, hv);
        CBAR();
        const v4u a0 = *(const LAS v4u*)(L + a_1r), a1 = *(const LAS v4u*)(L + a_1r + 64);
        f32x4 t4[4];
#pragma unroll
        for (int k = 0; k < 4; ++k) t4[k] = mfma(f2[4 + k], a1, mfma(f2[k], a0, z));
        f32x4 ts = (n & 2) ? ((n & 1) ? t4[3] : t4[2]) : ((n & 1) ? t4[1] : t4[0]);
        asm volatile("" : "+v"(ts));
        if (pass) {
          stsc(asu(ts), rws, sv, CO_OFF + j3 * 2048 + w * 256);
          if (w == 0) STAMP(15);
        }
      }
      if (j3 < 16 && w == 0) {
        const int oc = 32 * j3 + 4 * (lane & 7) + g;
        const float xr = ((const LAS float*)(L + L_X1))[oc], gr = lo16(md[1536 + oc]);
        v4u cp[4] = {};
        for (unsigned spins = 0; pass; ++spins) {
          CBAR();
#pragma unroll
          for (int i = 0; i < 4; ++i) cp[i] = ldsc(rws, (lane >> 3) * 2048 + (lane & 7) * 16, CO_OFF + i * 16384 + j3 * 128);
          if (!__any(umax(umax(umax4(cp[0]), umax4(cp[1])), umax(umax4(cp[2]), umax4(cp[3]))) == SENT) || spins > SPIN_CAP) break;
        }
        if (pass) STAMP(16);
        const f32x4 cs = (asf(cp[0]) + asf(cp[1])) + (asf(cp[2]) + asf(cp[3]));
        float os = row_tr(cs[0], cs[1], cs[2], cs[3]);
        os += dpp<ROR8>(os);
        const float o = xr + rbf(gr * rbf(os));
        unsigned ob = cvtpk(o, o);
        asm volatile("" : "+v"(ob));
        if (pass) {
          if (n < 8) ((GAS u16*)out)[oc] = (u16)ob;
          if (j3 == 0 && lane == 0) __hip_atomic_store((gu32*)(ws + EPOCH_OFF), tag, RLX_AGENT);
          STAMP(17);
        }
      }
    }
  }
}

extern "C" void kernel_launch(void* const* d_in, const int* in_sizes, int n_in, void* d_out, int out_size, void* d_ws, size_t ws_size,
                              hipStream_t stream) {
  (void)in_sizes; (void)n_in; (void)out_size; (void)ws_size;
  (void)hipFuncSetAttribute((const void*)fused_decode, hipFuncAttributeMaxDynamicSharedMemorySize, L_BYTES);
  fused_decode<<<NWG, NTHR, L_BYTES, stream>>>((const u16*)d_in[0], (const u16*)d_in[1], (const u16*)d_in[2], (const u16*)d_in[3], (const u16*)d_in[4],
                                               (const u16*)d_in[6], (const u16*)d_in[7], (const u16*)d_in[8], (const u16*)d_in[9], (u16*)d_out,
                                               (unsigned char*)d_ws);
}
```

```cpp
#include <hip/hip_runtime.h>
#include <stdint.h>

#define GAS __attribute__((address_space(1)))
#define LAS __attribute__((address_space(3)))
typedef unsigned v4u __attribute__((ext_vector_type(4)));
typedef unsigned v2u __attribute__((ext_vector_type(2)));
typedef float f32x4 __attribute__((ext_vector_type(4)));
typedef float f32x2 __attribute__((ext_vector_type(2)));
typedef short bf16x8 __attribute__((ext_vector_type(8)));
typedef __bf16 bf16x2 __attribute__((ext_vector_type(2)));
typedef unsigned short u16;
typedef unsigned long long u64;
typedef GAS unsigned gu32;
typedef GAS u64 gu64;
#define RLX_AGENT __ATOMIC_RELAXED, __HIP_MEMORY_SCOPE_AGENT

#define DIST0 2
#define DIST 2
#define DISTE 3
#define KEND 6
#define SIGK 6
#define SLP1 4
#define SLP2 8
#define E1 1
#define E2 0
#define E3 1
#define SIGPRE 1
#define PACE 1
#define C2LEAD 2
#define PBIAS 0.25f
#define PPRE 1
#define PBIASN 100.0f
#define PGAINN 1.5f
#define PROUNDS 4
#define PT0 1500
#define PTS 600
#define PTD 150
#define PFIRST 22
#define PSTEP 8
#define PLAST 6
#define PTAU 57
#define NPEER 32
#define PGAIN 4.0f
#define CODEPF 162
#define WPRE 44
#define MWAIT 520
#define QWAIT 880
#define SLPA 2
#define SLPB 2
#define TLBW 0
#define TLBN 2
#define T_G 64
#define T_PL 68
#define T_C3 60
#define T_C2 60
static_assert(16 * T_G + 8 * T_PL + 4 * T_C3 + 4 * T_C2 <= 2048, "tiles of one kv head");
constexpr int T_X = 2048 - (16 * T_G + 8 * T_PL + 4 * T_C3 + 4 * T_C2);
static_assert(T_X <= 8, "");

constexpr int D = 512, SK = 65536, DH = 128, FF = 1792;
constexpr int NWG = 256, NW = 8, NTHR = NW * 64;
constexpr int AUX_NT = 2, AUX_SC1 = 16;
constexpr unsigned SENT = 0xFFFFFFFFu;
constexpr unsigned SPIN_CAP = 1u << 16;
constexpr int OOB = 0x40000000;
constexpr int EPOCH_OFF = 0;
constexpr int PRG_OFF = 32768;
constexpr int MODG_OFF = 4096;
constexpr int QG_OFF = 16384;
constexpr int AO_OFF = 65536;
constexpr int BY_OFF = AO_OFF + 256 * 1024;
constexpr int CO_OFF = BY_OFF + 32 * 1024;
constexpr int WS_BYTES = CO_OFF + 28 * 2048;
constexpr int STAMP_OFF = 16 << 20;
constexpr int L_W = 0;
constexpr int L_MO = 131072;
constexpr int L_MM = L_MO + 16384;
constexpr int L_ML = L_MM + 128;
constexpr int L_Q = L_ML + 128;
constexpr int L_ACT = L_Q + 1024;
constexpr int L_RED = L_ACT + 1024;
constexpr int L_ST = L_RED + 4096;
constexpr int L_H1 = L_ST + 64;
constexpr int L_X1 = L_H1 + 1024;
constexpr int L_MOD = L_X1 + 2048;
constexpr int L_CTL = L_MOD + 5120;
constexpr int L_STAMP = L_CTL + 32;
constexpr int L_BYTES = L_STAMP;
static_assert(L_BYTES <= 160 * 1024, "LDS");

__device__ __forceinline__ float lo16(unsigned w) { return __builtin_bit_cast(float, w << 16); }
__device__ __forceinline__ float hi16(unsigned w) { return __builtin_bit_cast(float, w & 0xffff0000u); }
__device__ __forceinline__ unsigned cvtpk(float lo, float hi) {
  return __builtin_bit_cast(unsigned, __builtin_convertvector((f32x2{lo, hi}), bf16x2));
}
#define rbf(x) __builtin_bit_cast(float, __builtin_convertvector((f32x2{zero_, (x)}), bf16x2))
__device__ __forceinline__ f32x4 mfma(v4u a, v4u b, f32x4 c) {
  return __builtin_amdgcn_mfma_f32_16x16x32_bf16(__builtin_bit_cast(bf16x8, a), __builtin_bit_cast(bf16x8, b), c, 0, 0, 0);
}
__device__ __forceinline__ float dot2(unsigned a, unsigned b, float acc) {
  return __builtin_amdgcn_fdot2_f32_bf16(__builtin_bit_cast(bf16x2, a), __builtin_bit_cast(bf16x2, b), acc, false);
}
__device__ __forceinline__ float rcp(float x) { return __builtin_amdgcn_rcpf(x); }
__device__ __forceinline__ float ex2(float x) { return __builtin_amdgcn_exp2f(x); }
__device__ __forceinline__ float silu(float x) { return x * rcp(1.0f + ex2(x * -1.4426950408889634f)); }
template <int CTRL> __device__ __forceinline__ float dpp(float x) {
  return __builtin_bit_cast(float, __builtin_amdgcn_mov_dpp(__builtin_bit_cast(int, x), CTRL, 0xf, 0xf, true));
}
constexpr int XOR1 = 0xB1, XOR2 = 0x4E, HMIR = 0x141, ROR4 = 0x124, ROR8 = 0x128;
__device__ __forceinline__ float sum16(float x) {
  auto s = __builtin_amdgcn_permlane16_swap(__float_as_uint(x), __float_as_uint(x), false, false);
  return __uint_as_float(s[0]) + __uint_as_float(s[1]);
}
__device__ __forceinline__ float sum32(float x) {
  auto s = __builtin_amdgcn_permlane32_swap(__float_as_uint(x), __float_as_uint(x), false, false);
  return __uint_as_float(s[0]) + __uint_as_float(s[1]);
}
__device__ __forceinline__ float max16(float x) {
  auto s = __builtin_amdgcn_permlane16_swap(__float_as_uint(x), __float_as_uint(x), false, false);
  return fmaxf(__uint_as_float(s[0]), __uint_as_float(s[1]));
}
__device__ __forceinline__ float max32(float x) {
  auto s = __builtin_amdgcn_permlane32_swap(__float_as_uint(x), __float_as_uint(x), false, false);
  return fmaxf(__uint_as_float(s[0]), __uint_as_float(s[1]));
}
__device__ __forceinline__ float sum_q(float x) { x += dpp<XOR1>(x); x += dpp<XOR2>(x); return x; }
__device__ __forceinline__ float sum_8(float x) { x = sum_q(x); x += dpp<HMIR>(x); return x; }
__device__ __forceinline__ float sum_b23(float x) { x += dpp<ROR4>(x); x += dpp<ROR8>(x); return x; }
__device__ __forceinline__ float sum_row(float x) { return sum_b23(sum_q(x)); }
__device__ __forceinline__ float wave_sum(float x) { return sum32(sum16(sum_row(x))); }
__device__ __forceinline__ float quad_tr(f32x4 v, int lane) {
  const bool b0 = lane & 1, b1 = lane & 2;
  const float t0 = (b0 ? v[1] : v[0]) + dpp<XOR1>(b0 ? v[0] : v[1]);
  const float t1 = (b0 ? v[3] : v[2]) + dpp<XOR1>(b0 ? v[2] : v[3]);
  return (b1 ? t1 : t0) + dpp<XOR2>(b1 ? t0 : t1);
}
__device__ __forceinline__ float tr16(float a, float b) {
  auto s = __builtin_amdgcn_permlane16_swap(__float_as_uint(a), __float_as_uint(b), false, false);
  return __uint_as_float(s[0]) + __uint_as_float(s[1]);
}
__device__ __forceinline__ float tr32(float a, float b) {
  auto s = __builtin_amdgcn_permlane32_swap(__float_as_uint(a), __float_as_uint(b), false, false);
  return __uint_as_float(s[0]) + __uint_as_float(s[1]);
}
__device__ __forceinline__ float row_tr(float a, float b, float c, float d) { return tr32(tr16(a, b), tr16(c, d)); }
#define LGKM0() asm volatile("s_waitcnt lgkmcnt(0)" ::: "memory")
#define VM0() asm volatile("s_waitcnt vmcnt(0)" ::: "memory")
#define BAR() do { LGKM0(); __builtin_amdgcn_s_barrier(); asm volatile("" ::: "memory"); } while (0)
#define SCHED() __builtin_amdgcn_sched_barrier(0)
#define CBAR() asm volatile("" ::: "memory")
__device__ __forceinline__ unsigned umax(unsigned a, unsigned b) { return a > b ? a : b; }
__device__ __forceinline__ unsigned umax4(v4u v) { return umax(umax(v.x, v.y), umax(v.z, v.w)); }
__device__ __forceinline__ __amdgpu_buffer_rsrc_t mkrs(const void* p, int bytes) { return __builtin_amdgcn_make_buffer_rsrc((void*)p, 0, bytes, 0x00020000); }
__device__ __forceinline__ v4u ldnt(__amdgpu_buffer_rsrc_t r, int vo, int so) { return __builtin_amdgcn_raw_buffer_load_b128(r, vo, so, AUX_NT); }
__device__ __forceinline__ v4u ldsc(__amdgpu_buffer_rsrc_t r, int vo, int so) { return __builtin_amdgcn_raw_buffer_load_b128(r, vo, so, AUX_SC1); }
__device__ __forceinline__ void stsc(v4u v, __amdgpu_buffer_rsrc_t r, int vo, int so) { __builtin_amdgcn_raw_buffer_store_b128(v, r, vo, so, AUX_SC1); }
__device__ __forceinline__ f32x4 asf(v4u v) { return __builtin_bit_cast(f32x4, v); }
__device__ __forceinline__ v4u asu(f32x4 v) { return __builtin_bit_cast(v4u, v); }
#define PLO 0x05040100u
#define PHI 0x07060302u

#define STAMP(k) do {} while (0)

__global__ void __launch_bounds__(NTHR, 2)
fused_decode(const u16* __restrict__ xin, const u16* __restrict__ kc, const u16* __restrict__ vc, const u16* __restrict__ wq,
             const u16* __restrict__ wo, const u16* __restrict__ w1, const u16* __restrict__ w2, const u16* __restrict__ cin,
             const u16* __restrict__ wmod, u16* __restrict__ out, unsigned char* ws) {
  extern __shared__ __attribute__((aligned(16))) unsigned char smem[];
  const u64 t_entry = __builtin_amdgcn_s_memrealtime();
  unsigned pf0 = 0, pf1 = 0;
  {
    const GAS unsigned char* pc = (const GAS unsigned char*)(__builtin_amdgcn_s_getpc() & ~127ull);
    if (threadIdx.x < CODEPF) pf0 = *(const GAS unsigned*)(pc + threadIdx.x * 128);
  }
  LAS unsigned char* L = (LAS unsigned char*)smem;
  volatile LAS unsigned* ctl = (volatile LAS unsigned*)(L + L_CTL);
  const int w_0 = __builtin_amdgcn_readfirstlane(threadIdx.x >> 6);
#define INDICES() \
  int lane = threadIdx.x & 63, w = w_0, b = blockIdx.x; \
  asm volatile("" : "+v"(lane)); asm volatile("" : "+s"(w)); asm volatile("" : "+s"(b)); \
  float zero_ = 0.f; asm volatile("" : "+v"(zero_)); \
  const int g = lane >> 4, n = lane & 15, xg = b & 7, ig = b >> 3; \
  const bool isC2 = ig >= 28, isC3 = ig >= 24 && ig < 28 && xg < 7, isC4 = (ig >> 1) == 8; \
  const int hp = 2 * xg + ((ig >> 1) & 1), cq = ig & 1;         \
  const int j3 = 4 * xg + (ig - 24);                            \
  (void)g; (void)n; (void)isC2; (void)isC3; (void)isC4; (void)hp; (void)cq; (void)j3;
  asm volatile("" ::"s"(xin), "s"(kc), "s"(vc), "s"(wq), "s"(wo), "s"(w1), "s"(w2), "s"(cin), "s"(wmod), "s"(out), "s"(ws));
  const __amdgpu_buffer_rsrc_t rws = mkrs(ws, WS_BYTES);
  if (threadIdx.x == 0) { ctl[0] = 0u; ctl[2] = 0u; ctl[3] = 0u; ctl[4] = 0u; }
  BAR();
  LAS float* red = (LAS float*)(L + L_RED);
#define NTILES (ig < 16 ? T_G : ig < 16 + T_X ? T_PL + 1 : ig < 24 ? T_PL : ig < 28 ? T_C3 : T_C2)
#define TILE0 (NTILES * ig + (ig < 16 ? 0 : ig < 16 + T_X ? 16 * (T_G - T_PL - 1) : ig < 24 ? 16 * (T_G - T_PL) + T_X : ig < 28 ? 2048 - 4 * T_C2 - 28 * T_C3 : 2048 - 32 * T_C2))
#define GCOL (ig < 8 ? 1024 + 256 * xg + 64 * (ig - 4) : 512 * xg + 64 * (ig - 8))
#define TAG() (__hip_atomic_load((gu32*)(ws + EPOCH_OFF), RLX_AGENT) + 1u)
  unsigned tag0 = 0; v4u in8 = {};
  {
    INDICES();
    if (w == 0) {
      STAMP(1);
      tag0 = __hip_atomic_load((gu32*)(ws + EPOCH_OFF), RLX_AGENT);
#pragma unroll
      for (int d = 0; d < 4; ++d) in8[d] = ((const GAS unsigned*)(ig < 8 ? cin : xin))[lane + 64 * d];
    }
    if (ig < 4) {
      const __amdgpu_buffer_rsrc_t rm = mkrs(wmod, D * 6144);
      const int c0 = 128 * xg + 32 * ig;
      v4u R[4];
#pragma unroll
      for (int j = 0; j < 4; ++j) R[j] = ldnt(rm, (4 * (lane >> 2)) * 6144 + (lane & 3) * 16, (64 * w + j) * 6144 + c0 * 2);
      const v2u c4 = *(const GAS v2u*)((const GAS u16*)cin + 64 * w + 4 * (lane >> 2));
      stsc(v4u{SENT, SENT, SENT, SENT}, rws, lane < 32 ? lane * 16 : OOB, BY_OFF + ((4 * xg + ig) * 8 + w) * 512);
      SCHED();
      const unsigned a0 = cvtpk(silu(lo16(c4.x)), silu(hi16(c4.x))), a1 = cvtpk(silu(lo16(c4.y)), silu(hi16(c4.y)));
      float acc[8];
#pragma unroll
      for (int d = 0; d < 4; ++d) {
        acc[2 * d] = dot2(__builtin_amdgcn_perm(R[3][d], R[2][d], PLO), a1, dot2(__builtin_amdgcn_perm(R[1][d], R[0][d], PLO), a0, 0.f));
        acc[2 * d + 1] = dot2(__builtin_amdgcn_perm(R[3][d], R[2][d], PHI), a1, dot2(__builtin_amdgcn_perm(R[1][d], R[0][d], PHI), a0, 0.f));
      }
      const float t0 = sum_b23(row_tr(acc[0], acc[2], acc[4], acc[6])), t1 = sum_b23(row_tr(acc[1], acc[3], acc[5], acc[7]));
      *(LAS f32x2*)(red + w * 32 + (n & 3) * 8 + g * 2) = f32x2{t0, t1};
      VM0();
      BAR();
      if (w == 0 && lane < 16) {
        f32x2 t = {0.f, 0.f};
#pragma unroll
        for (int i = 0; i < NW; ++i) t += *(const LAS f32x2*)(red + i * 32 + 2 * lane);
        __hip_atomic_store((gu64*)(ws + MODG_OFF) + (c0 >> 1) + lane, ((u64)(tag0 + 1u) << 32) | cvtpk(t.x, t.y), RLX_AGENT);
      }
      if (w == 0) STAMP(4);
    } else if (ig < 16 || isC2 || isC3) {
      const int ro = lane >> 3, c8 = lane & 7;
      const bool one = ig < 16;
      v4u R[2][8];
#pragma unroll
      for (int h = 0; h < 2; ++h) {
        const int rs = (isC2 || (isC3 && h)) ? D * 2 : isC3 ? FF * 2 : ig < 8 ? 6144 : 8192;
        const __amdgpu_buffer_rsrc_t r = mkrs(isC2 ? wo : isC3 ? (h ? w2 : w1) : ig < 8 ? wmod : wq, (one && h) ? 0 : isC2 ? 4096 * D * 2 : isC3 ? D * FF * 2 : D * rs);
        const int vo = isC2 ? (8 * g) * (D * 2) + n * 16 : (8 * ro) * rs + c8 * 16;
        const int so = isC2 ? (256 * hp + 32 * w) * (D * 2) + cq * 512 + h * 256 : (isC3 && h) ? 64 * j3 * (D * 2) + w * 128 : 64 * w * rs + (isC3 ? 64 * j3 : GCOL) * 2;
#pragma unroll
        for (int u = 0; u < 8; ++u) R[h][u] = ldnt(r, vo, so + u * rs);
      }
      stsc(v4u{SENT, SENT, SENT, SENT}, rws, (ig >= 8 && one && lane < 32) ? lane * 16 : OOB, AO_OFF + xg * 32768 + ((ig & 7) * 8 + w) * 512);
      SCHED();
      const int la = isC2 ? (((n >> 1) * 8 + w) * 64 + g * 16 + 8 * (n & 1)) * 16 : (((w * 2 + (ro >> 2)) * 4 + (c8 >> 1)) * 64 + (ro & 3) * 16 + 8 * (c8 & 1)) * 16;
#pragma unroll
      for (int h = 0; h < 2; ++h) {
        if (h && one) break;
#pragma unroll
        for (int e = 0; e < 8; ++e) {
          v4u u;
#pragma unroll
          for (int jj = 0; jj < 4; ++jj) u[jj] = __builtin_amdgcn_perm(R[h][2 * jj + 1][e >> 1], R[h][2 * jj][e >> 1], (e & 1) ? PHI : PLO);
          *(LAS v4u*)(L + L_W + h * 65536 + la + e * 16) = u;
        }
      }
      VM0();
      LGKM0();
      if (lane == 0) __hip_atomic_fetch_add((LAS unsigned*)(L + L_CTL + 16), 1u, __ATOMIC_RELAXED, __HIP_MEMORY_SCOPE_WORKGROUP);
      if (w == 0) STAMP(2);
    }
  }
  if (w_0 == 0) {
    INDICES();
    __builtin_amdgcn_s_setprio(3);
    const unsigned tag = tag0 + 1u;
    if (ig >= 4 && ig < 16) {
      const bool isq = ig >= 8;
      for (unsigned spins = 0; __builtin_amdgcn_readfirstlane(ctl[4]) < 8u && spins < (1u << 22); ++spins) __builtin_amdgcn_s_sleep(1);
      CBAR();
      v4u wf[WPRE];
#pragma unroll
      for (int k = 0; k < WPRE; ++k) { wf[k] = *(const LAS v4u*)(L + L_W + (k * 64 + lane) * 16); }
#pragma unroll
      for (int k = 0; k < WPRE; ++k) asm volatile("" : "+v"(wf[k]));
      float xv[8];
#pragma unroll
      for (int d = 0; d < 4; ++d) { xv[2 * d] = lo16(in8[d]); xv[2 * d + 1] = hi16(in8[d]); }
      if (isq) {
        const float mean = wave_sum(((xv[0] + xv[1]) + (xv[2] + xv[3])) + ((xv[4] + xv[5]) + (xv[6] + xv[7]))) * (1.0f / D);
        float sq = 0.f;
#pragma unroll
        for (int e = 0; e < 8; ++e) sq += (xv[e] - mean) * (xv[e] - mean);
        const float var = rbf(wave_sum(sq) * (1.0f / D));
        const float mb = rbf(mean), isd = rcp(rbf(__builtin_amdgcn_sqrtf(rbf(var + 1e-5f))));
#pragma unroll
        for (int e = 0; e < 8; ++e) xv[e] = rbf(rbf(xv[e] - mb) * isd);
        for (unsigned spins = 0; __builtin_amdgcn_s_memrealtime() - t_entry < MWAIT && spins < 1024; ++spins) __builtin_amdgcn_s_sleep(2);
        const gu64* gs = (const gu64*)(ws + MODG_OFF) + lane;
        u64 v[8];
        for (unsigned spins = 0;; ++spins) {
          bool ok = true;
#pragma unroll
          for (int d = 0; d < 8; ++d) v[d] = __hip_atomic_load((gu64*)gs + 64 * d, RLX_AGENT);
#pragma unroll
          for (int d = 0; d < 8; ++d) ok &= (unsigned)(v[d] >> 32) == tag;
          if (__all(ok) || spins > SPIN_CAP) break;
          __builtin_amdgcn_s_sleep(SLP1);
        }
        STAMP(3);
#pragma unroll
        for (int d = 0; d < 4; ++d) {
          const unsigned sa = (unsigned)v[d], ca = (unsigned)v[4 + d];
          xv[2 * d] = rbf(xv[2 * d] * rbf(1.0f + lo16(ca))) + lo16(sa);
          xv[2 * d + 1] = rbf(xv[2 * d + 1] * rbf(1.0f + hi16(ca))) + hi16(sa);
        }
      } else {
#pragma unroll
        for (int e = 0; e < 8; ++e) xv[e] = silu(xv[e]);
      }
#pragma unroll
      for (int d = 0; d < 4; ++d) ((LAS unsigned*)(L + L_ACT))[lane + 64 * d] = cvtpk(xv[2 * d], xv[2 * d + 1]);
      CBAR();
      f32x4 acc[4] = {};
#pragma unroll
      for (int k = 0; k < 16; ++k) {
        const v4u a = *(const LAS v4u*)(L + L_ACT + k * 64 + g * 16);
#pragma unroll
        for (int t = 0; t < 4; ++t) acc[t] = mfma(k * 4 + t < WPRE ? wf[(k * 4 + t) % WPRE] : *(const LAS v4u*)(L + L_W + ((k * 4 + t) * 64 + lane) * 16), a, acc[t]);
      }
      const f32x4 ts = (n & 4) ? ((n & 2) ? acc[3] : acc[2]) : ((n & 2) ? acc[1] : acc[0]);
      if (n < 8)
        __hip_atomic_store((gu64*)(ws + (isq ? QG_OFF : MODG_OFF)) + (GCOL >> 1) + 8 * (n >> 1) + 2 * g + (n & 1),
                           ((u64)tag << 32) | ((n & 1) ? cvtpk(ts[2], ts[3]) : cvtpk(ts[0], ts[1])), RLX_AGENT);
      if (isq) STAMP(5);
    }
    for (unsigned spins = 0; __builtin_amdgcn_s_memrealtime() - t_entry < QWAIT && spins < 1024; ++spins) __builtin_amdgcn_s_sleep(4);
    {
      const gu64* gs = (const gu64*)(ws + QG_OFF) + 256 * xg + lane;
      u64 v[4];
      for (unsigned spins = 0;; ++spins) {
        bool ok = true;
#pragma unroll
        for (int d = 0; d < 4; ++d) v[d] = __hip_atomic_load((gu64*)gs + 64 * d, RLX_AGENT);
#pragma unroll
        for (int d = 0; d < 4; ++d) ok &= (unsigned)(v[d] >> 32) == tag;
        if (__all(ok) || spins > SPIN_CAP) break;
        __builtin_amdgcn_s_sleep(SLP2);
      }
#pragma unroll
      for (int d = 0; d < 4; ++d) ((LAS unsigned*)(L + L_Q))[64 * d + lane] = (unsigned)v[d];
      ctl[1] = tag;
      LGKM0();
      ctl[0] = 1u;
      STAMP(6);
    }
    __builtin_amdgcn_s_setprio(0);
    if (isC3 || isC4) {
      const gu64* gm = (const gu64*)(ws + MODG_OFF) + 512 + lane;
      u64 t[16];
      for (unsigned spins = 0;; ++spins) {
        bool ok = true;
#pragma unroll
        for (int d = 0; d < 16; ++d) t[d] = __hip_atomic_load((gu64*)gm + 64 * d, RLX_AGENT);
#pragma unroll
        for (int d = 0; d < 16; ++d) ok &= (unsigned)(t[d] >> 32) == tag;
        if (__all(ok) || spins > SPIN_CAP) break;
      }
#pragma unroll
      for (int d = 0; d < 16; ++d) ((LAS unsigned*)(L + L_MOD))[64 * d + lane] = (unsigned)t[d];
#pragma unroll
      for (int d = 0; d < 4; ++d) ((LAS unsigned*)(L + L_MOD + 4096))[lane + 64 * d] = in8[d];
    }
    *(LAS f32x4*)(L + L_MO + lane * 32) = f32x4{0.f, 0.f, 0.f, 0.f};
    *(LAS f32x4*)(L + L_MO + lane * 32 + 16) = f32x4{0.f, 0.f, 0.f, 0.f};
    if (lane < 4) { ((LAS float*)(L + L_MM))[lane * 8] = -1e30f; ((LAS float*)(L + L_ML))[lane * 8] = 0.f; }
    STAMP(7);
    {
      const int nt = NTILES + (isC2 ? C2LEAD : 0);
#pragma unroll 1
      for (int rd = 0; rd < PROUNDS; ++rd) {
        const unsigned tp = PT0 + rd * PTS;
        for (unsigned spins = 0; __builtin_amdgcn_s_memrealtime() - t_entry < tp && spins < 4096; ++spins) __builtin_amdgcn_s_sleep(4);
        const int rem = nt - (int)__builtin_amdgcn_readfirstlane(ctl[2]);
        if (rem <= 6) break;
        if (lane == 0) __hip_atomic_store((gu64*)(ws + PRG_OFF) + rd * 256 + b, ((u64)tag << 32) | (unsigned)rem, RLX_AGENT);
        for (unsigned spins = 0; __builtin_amdgcn_s_memrealtime() - t_entry < tp + PTD && spins < 4096; ++spins) __builtin_amdgcn_s_sleep(4);
        if (PPRE) ctl[3] = ctl[2] + 6;
        CBAR();
        const v4u pa = ldsc(rws, lane * 16, PRG_OFF + rd * 2048), pb = ldsc(rws, lane * 16, PRG_OFF + rd * 2048 + 1024);
        const float cnt = wave_sum((pa.y == tag ? 1.f : 0.f) + (pa.w == tag ? 1.f : 0.f) + (pb.y == tag ? 1.f : 0.f) + (pb.w == tag ? 1.f : 0.f));
        const float sum = wave_sum((pa.y == tag ? (float)pa.x : 0.f) + (pa.w == tag ? (float)pa.z : 0.f) + (pb.y == tag ? (float)pb.x : 0.f) + (pb.w == tag ? (float)pb.z : 0.f));
        const float lag = (float)rem - sum * rcp(fmaxf(cnt, 1.f));
        const int here = (int)ctl[2] + 2;
        ctl[3] = (unsigned)(lag > PBIAS ? here + (int)(PGAIN * (lag - PBIAS)) : lag < -PBIASN ? -(here + (int)(PGAINN * (-lag - PBIASN))) : 0);
      }
    }
  } else {
    INDICES();
    constexpr float C2 = 0.088388347648318440f * 1.4426950408889634f;
    const int nt = NTILES;
    const int t0 = TILE0;
    const __amdgpu_buffer_rsrc_t rk = mkrs(kc + ((size_t)xg * SK + (size_t)t0 * 32) * DH, nt * 8192);
    const __amdgpu_buffer_rsrc_t rv = mkrs(vc + ((size_t)xg * SK + (size_t)t0 * 32) * DH, nt * 8192);
    int koff = (4 * (n & 3) + ((n >> 2) & 1)) * 256 + (n >> 3) * 64 + 16 * g, voff = lane * 16;
    v4u kf[3][8], vr[3][8];
    int id = w - 1;
#define ISSUE(d, dist) { \
      for (unsigned spins = 0; spins < (1u << 22); ++spins) { \
        const u64 c_ = *(volatile LAS u64*)(L + L_CTL + 8); \
        const int u_ = (int)__builtin_amdgcn_readfirstlane((unsigned)(c_ >> 32));        \
        if ((int)__builtin_amdgcn_readfirstlane((unsigned)c_) + (dist) + (id < u_ ? 1 : id < -u_ ? -1 : 0) > id) break; \
        __builtin_amdgcn_s_sleep(1); } \
      const int so_ = id * 8192; asm volatile("" : "+v"(koff), "+v"(voff) :: "memory"); \
      _Pragma("unroll") for (int u = 0; u < 8; ++u) kf[d][u] = ldnt(rk, koff + ((u >> 1) & 1) * 512 + (u & 1) * 128, so_ + (u >> 2) * 4096); \
      _Pragma("unroll") for (int u = 0; u < 8; ++u) vr[d][u] = ldnt(rv, voff + (u & 3) * 1024, so_ + (u >> 2) * 4096); \
      SCHED(); }
#define SIGNAL() { \
      SCHED(); \
      asm volatile("s_waitcnt vmcnt(%0)" ::"i"(SIGK) : "memory"); \
      if (lane == 0) __hip_atomic_fetch_add((LAS unsigned*)(L + L_CTL + 8), 1u, __ATOMIC_RELAXED, __HIP_MEMORY_SCOPE_WORKGROUP); \
      STAMP(32 + id); \
      SCHED(); \
      id += 7; }
#define QK(d) { \
      f32x4 sq[4]; \
      _Pragma("unroll") for (int t = 0; t < 4; ++t) sq[t] = mfma(kf[d][2 * t + 1], qf[1], mfma(kf[d][2 * t], qf[0], f32x4{0.f, 0.f, 0.f, 0.f})); \
      float sc[2][4]; \
      _Pragma("unroll") for (int j = 0; j < 8; ++j) { \
        auto x_ = __builtin_amdgcn_permlane32_swap(__float_as_uint(sq[2 * (j >> 2)][j & 3]), __float_as_uint(sq[2 * (j >> 2) + 1][j & 3]), false, false); \
        sc[j >> 2][j & 3] = __uint_as_float(x_[0]) + dpp<ROR4>(__uint_as_float(x_[1])); } \
      float tm = fmaxf(fmaxf(fmaxf(sc[0][0], sc[0][1]), fmaxf(sc[0][2], sc[0][3])), fmaxf(fmaxf(sc[1][0], sc[1][1]), fmaxf(sc[1][2], sc[1][3]))); \
      tm = max32(max16(tm)); \
      const float mn = fmaxf(m, tm * C2); \
      const float alpha = ex2(m - mn); \
      m = mn; \
      float p[8]; \
      _Pragma("unroll") for (int j = 0; j < 8; ++j) p[j] = ex2(sc[j >> 2][j & 3] * C2 - mn); \
      l = l * alpha + (((p[0] + p[1]) + (p[2] + p[3])) + ((p[4] + p[5]) + (p[6] + p[7]))); \
      pf = v4u{cvtpk(p[0], p[1]), cvtpk(p[2], p[3]), cvtpk(p[4], p[5]), cvtpk(p[6], p[7])}; \
      _Pragma("unroll") for (int e = 0; e < 8; ++e) acc[e] *= alpha; \
      asm volatile("" : "+v"(pf), "+v"(l), "+v"(acc[0]), "+v"(acc[1]), "+v"(acc[2]), "+v"(acc[3]), "+v"(acc[4]), "+v"(acc[5]), "+v"(acc[6]), "+v"(acc[7])); }
#define PV(d) { \
      _Pragma("unroll") for (int e = 0; e < 8; ++e) { \
        v4u bf; \
        _Pragma("unroll") for (int jj = 0; jj < 4; ++jj) \
          bf[jj] = __builtin_amdgcn_perm(vr[d][2 * jj + 1][e >> 1], vr[d][2 * jj][e >> 1], (e & 1) ? PHI : PLO); \
        acc[e] = mfma(bf, pf, acc[e]); } \
      SCHED(); }
    ISSUE(2, DIST0) SIGNAL() ISSUE(1, DIST0) SIGNAL() ISSUE(0, DIST0) SIGNAL()
    for (unsigned spins = 0; __builtin_amdgcn_readfirstlane(ctl[0]) == 0u && spins < (1u << 22); ++spins) __builtin_amdgcn_s_sleep(1);
    CBAR();
    v4u qf[2], pf;
#pragma unroll
    for (int s = 0; s < 2; ++s) qf[s] = *(const LAS v4u*)(L + L_Q + (n & 3) * 256 + 128 * s + 64 * ((n >> 2) & 1) + 16 * g);
    f32x4 acc[8];
#pragma unroll
    for (int e = 0; e < 8; ++e) acc[e] = f32x4{0.f, 0.f, 0.f, 0.f};
    float m = -1e30f, l = 0.f;
    if (w == 1) STAMP(8);
    QK(2) PV(2) QK(1) PV(1) QK(0)
#pragma unroll 1
    for (;;) {
      PV(0)
      if (id >= nt) break;
      ISSUE(0, (id >= nt - KEND ? DISTE : DIST))
      SIGNAL()
      QK(0)
    }
    l = sum32(sum16(l));
    if (n < 4) {
#pragma unroll
      for (int e = 0; e < 8; ++e) *(LAS f32x4*)(L + L_MO + ((w * 4 + n) * 128 + 32 * g + 4 * e) * 4) = acc[e];
      ((LAS float*)(L + L_MM))[n * 8 + w] = m; ((LAS float*)(L + L_ML))[n * 8 + w] = l;
    }
  }
  asm volatile("" ::"v"(pf0), "v"(pf1));
  {
    constexpr bool pass = true;
#define PBAR() BAR()
    INDICES();
    const unsigned tag = ctl[1];
    {
      const int r = w >> 1, sub = lane & 3;
      int a_m = L_MM + r * 32 + sub * 8, a_o = L_MO + (((2 * sub) * 4 + r) * 128 + (w & 1) * 64 + 4 * (lane >> 2)) * 4,
          sv = sub == 0 ? AO_OFF + (lane >> 2) * 8 : OOB, sh = (lane >> 2) * 2;
      asm volatile("" : "+v"(a_m), "+v"(a_o), "+v"(sv), "+v"(sh));
      PBAR();
      if (w == 0 && pass) STAMP(10);
      const f32x2 mo = *(const LAS f32x2*)(L + a_m), lo = *(const LAS f32x2*)(L + a_m + 128);
      const f32x4 oa = *(const LAS f32x4*)(L + a_o), ob = *(const LAS f32x4*)(L + a_o + 2048);
      float M = fmaxf(mo.x, mo.y);
      M = fmaxf(M, dpp<XOR1>(M)); M = fmaxf(M, dpp<XOR2>(M));
      const float wa = ex2(mo.x - M), wb = ex2(mo.y - M);
      const float Ls = sum_q(wa * lo.x + wb * lo.y);
      const float inv = rcp(Ls);
      const unsigned lse = __float_as_uint(M + __builtin_amdgcn_logf(Ls)) >> sh;
      f32x4 o = wa * oa + wb * ob;
#pragma unroll
      for (int e = 0; e < 4; ++e) o[e] = sum_q(o[e]) * inv;
      v2u pk = {(cvtpk(o[0], o[1]) & ~1u) | (lse & 1u), (cvtpk(o[2], o[3]) & ~1u) | ((lse >> 1) & 1u)};
      asm volatile("" : "+v"(pk));
      if (pass) {
        __builtin_amdgcn_raw_buffer_store_b64(pk, rws, sv, (xg * 32 + ig) * 1024 + w * 128, AUX_SC1);
        if (w == 0) STAMP(11);
      }
    }
    if (isC2) {
      v4u wf[16];
#pragma unroll
      for (int k = 0; k < 16; ++k) wf[k] = *(const LAS v4u*)(L + L_W + ((w * 16 + k) * 64 + lane) * 16);
      const int dc = lane & 7;
      int aw = L_ACT + (w & 1) * 512 + ((w >> 1) * 64 + 32 * (dc >> 2) + 8 * g + 2 * (dc & 3)) * 2, ar = L_ACT + ((n >> 1) & 1) * 512 + g * 16, sv = n < 2 ? n * 64 + g * 16 : OOB,
          vo = (lane >> 3) * 1024 + dc * 16, sh = 4 * dc;
      asm volatile("" : "+v"(aw), "+v"(ar), "+v"(sv), "+v"(vo), "+v"(sh));
      v4u o4[2] = {};
      for (unsigned spins = 0;; ++spins) {
        CBAR();
#pragma unroll
        for (int i = 0; i < 2; ++i) o4[i] = ldsc(rws, vo, AO_OFF + xg * 32768 + (w & 1) * 16384 + i * 8192 + (hp & 1) * 512 + (w >> 1) * 128);
        if (!__any(umax(umax4(o4[0]), umax4(o4[1])) == SENT) || spins > SPIN_CAP) break;
        if (SLPA) __builtin_amdgcn_s_sleep(SLPA);
      }
      if (w == 0) STAMP(12);
      float ls[2];
#pragma unroll
      for (int i = 0; i < 2; ++i) {
        int t = (int)((((o4[i].x & 1u) | ((o4[i].y & 1u) << 1)) | (((o4[i].z & 1u) << 2) | ((o4[i].w & 1u) << 3))) << sh);
        t |= __builtin_amdgcn_mov_dpp(t, XOR1, 0xf, 0xf, true); t |= __builtin_amdgcn_mov_dpp(t, XOR2, 0xf, 0xf, true); t |= __builtin_amdgcn_mov_dpp(t, HMIR, 0xf, 0xf, true);
        ls[i] = __builtin_bit_cast(float, t);
      }
      float M = fmaxf(ls[0], ls[1]);
      M = fmaxf(M, dpp<ROR8>(M)); M = max32(max16(M));
      const float wa = ex2(ls[0] - M), wb = ex2(ls[1] - M);
      float S = wa + wb;
      S += dpp<ROR8>(S); S = sum32(sum16(S));
      float o[8];
#pragma unroll
      for (int d = 0; d < 4; ++d) { o[2 * d] = wa * lo16(o4[0][d]) + wb * lo16(o4[1][d]); o[2 * d + 1] = wa * hi16(o4[0][d]) + wb * hi16(o4[1][d]); }
      float t0 = row_tr(o[0], o[1], o[2], o[3]), t1 = row_tr(o[4], o[5], o[6], o[7]);
      t0 += dpp<ROR8>(t0); t1 += dpp<ROR8>(t1);
      *(LAS unsigned*)(L + aw) = cvtpk(t0, t1);
      *(LAS f32x2*)(L + L_ST + w * 8) = f32x2{M, S};
      PBAR();
      f32x4 y[4] = {};
#pragma unroll
      for (int k = 0; k < 4; ++k) {
        const v4u a0 = *(const LAS v4u*)(L + ar + k * 64), a1 = *(const LAS v4u*)(L + ar + (4 + k) * 64);
        y[0] = mfma(wf[k], a0, y[0]); y[1] = mfma(wf[4 + k], a1, y[1]);
        y[2] = mfma(wf[8 + k], a0, y[2]); y[3] = mfma(wf[12 + k], a1, y[3]);
      }
      float f[2];
#pragma unroll
      for (int h = 0; h < 2; ++h) {
        const f32x4 t = *(const LAS f32x4*)(L + L_ST + h * 32);
        const float Mh = fmaxf(t[0], t[2]), ea = ex2(t[0] - Mh), eb = ex2(t[2] - Mh);
        f[h] = ((n & 2) ? eb : ea) * rcp(ea * t[1] + eb * t[3]);
      }
      f32x4 ys = f[0] * ((n & 1) ? y[2] : y[0]) + f[1] * ((n & 1) ? y[3] : y[1]);
#pragma unroll
      for (int e = 0; e < 4; ++e) ys[e] += dpp<XOR2>(ys[e]);
      asm volatile("" : "+v"(ys));
      if (pass) {
        stsc(asu(ys), rws, sv, BY_OFF + hp * 2048 + cq * 1024 + w * 128);
        if (w == 0) STAMP(13);
      }
    } else if (isC3) {
      v4u f1[8], f2[8];
#pragma unroll
      for (int k = 0; k < 8; ++k) f1[k] = *(const LAS v4u*)(L + L_W + ((w * 8 + k) * 64 + lane) * 16);
#pragma unroll
      for (int k = 0; k < 8; ++k) f2[k] = *(const LAS v4u*)(L + L_W + 65536 + ((w * 8 + k) * 64 + lane) * 16);
      const int col = 64 * w + 4 * n + g;
      const LAS u16* md = (const LAS u16*)(L + L_MOD);
      float ga = lo16(md[col]), cb = rbf(1.0f + lo16(md[1024 + col])), xv = lo16(md[2048 + col]);
      int a_st = L_ST + w * 8 + (lane >> 5) * 4, a_hw = L_ACT + col * 2, a_hr = L_ACT + w * 128 + g * 16, a_rw = L_RED + w * 256 + (n & 3) * 64 + g * 16,
          a_rr = L_RED + lane * 4, a_1w = L_H1 + w * 128 + lane * 2, a_1r = L_H1 + w * 128 + g * 16, sv = n < 4 ? n * 64 + g * 16 : OOB;
      const f32x4 z = {0.f, 0.f, 0.f, 0.f};
#define W1_PARTIAL(src) { \
        const v4u a0 = *(const LAS v4u*)(src), a1 = *(const LAS v4u*)((src) + 64); \
        f32x4 t4[4]; \
        _Pragma("unroll") for (int k = 0; k < 4; ++k) t4[k] = mfma(f1[4 + k], a1, mfma(f1[k], a0, z)); \
        ts = (n & 2) ? ((n & 1) ? t4[3] : t4[2]) : ((n & 1) ? t4[1] : t4[0]); }
#define RED_SUM(rr) (((rr[0] + rr[64]) + (rr[128] + rr[192])) + ((rr[256] + rr[320]) + (rr[384] + rr[448])))
      f32x4 ts;
      *(LAS u16*)(L + a_hw) = (u16)(__float_as_uint(cb) >> 16);
      CBAR();
      W1_PARTIAL(L + ((n & 4) ? L_MOD + 1024 + w * 128 + g * 16 : a_hr))
      *(LAS f32x4*)(L + a_rw + ((n & 4) ? 2048 : 0)) = ts;
      PBAR();
      float cw = RED_SUM(((const LAS float*)(L + a_rr))), sw = RED_SUM(((const LAS float*)(L + a_rr + 2048)));
      asm volatile("" : "+v"(ga), "+v"(cb), "+v"(xv), "+v"(cw), "+v"(sw), "+v"(a_st), "+v"(a_hw), "+v"(a_hr), "+v"(a_rw), "+v"(a_rr), "+v"(a_1w), "+v"(a_1r), "+v"(sv));
      PBAR();
      v4u yp[4] = {};
      for (unsigned spins = 0; pass; ++spins) {
        CBAR();
#pragma unroll
        for (int i = 0; i < 4; ++i) yp[i] = ldsc(rws, g * 2048 + n * 16, BY_OFF + i * 8192 + w * 256);
        if (!__any(umax(umax(umax4(yp[0]), umax4(yp[1])), umax(umax4(yp[2]), umax4(yp[3]))) == SENT) || spins > SPIN_CAP) break;
        if (SLPB) __builtin_amdgcn_s_sleep(SLPB);
      }
      if (w == 0 && pass) STAMP(14);
      const f32x4 ys = (asf(yp[0]) + asf(yp[1])) + (asf(yp[2]) + asf(yp[3]));
      const float x1 = rbf(xv + rbf(ga * rbf(row_tr(ys[0], ys[1], ys[2], ys[3]))));
      *(LAS u16*)(L + a_hw) = (u16)cvtpk(x1 * cb, x1);
      CBAR();
      W1_PARTIAL(L + a_hr)
      *(LAS float*)(L + a_st) = sum_row(sum16(tr32(x1, x1 * x1)));
      *(LAS f32x4*)(L + a_rw) = ts;
      PBAR();
      {
        const f32x4 t = (*(const LAS f32x4*)(L + L_ST) + *(const LAS f32x4*)(L + L_ST + 16)) + (*(const LAS f32x4*)(L + L_ST + 32) + *(const LAS f32x4*)(L + L_ST + 48));
        const float mean = (t[0] + t[2]) * (1.0f / D);
        const float var = rbf(fmaxf((t[1] + t[3]) * (1.0f / D) - mean * mean, 0.f));
        const float isd = rcp(rbf(__builtin_amdgcn_sqrtf(rbf(var + 1e-5f))));
        const float hv = silu(rbf((RED_SUM(((const LAS float*)(L + a_rr))) - rbf(mean) * cw) * isd + sw));
        *(LAS u16*)(L + a_1w) = (u16)cvtpk(hv, hv);
        CBAR();
        const v4u a0 = *(const LAS v4u*)(L + a_1r), a1 = *(const LAS v4u*)(L + a_1r + 64);
        f32x4 t4[4];
#pragma unroll
        for (int k = 0; k < 4; ++k) t4[k] = mfma(f2[4 + k], a1, mfma(f2[k], a0, z));
        ts = (n & 2) ? ((n & 1) ? t4[3] : t4[2]) : ((n & 1) ? t4[1] : t4[0]);
        asm volatile("" : "+v"(ts));
        if (pass) {
          stsc(asu(ts), rws, sv, CO_OFF + j3 * 2048 + w * 256);
          if (w == 0) STAMP(15);
        }
      }
    } else if (isC4 && w == 0) {
      const int jc = 2 * xg + (ig & 1), oc = 32 * jc + 4 * (lane & 7) + g, vo = (lane >> 3) * 2048 + (lane & 7) * 16;
      const LAS u16* md = (const LAS u16*)(L + L_MOD);
      const float ga = lo16(md[oc]), gb = lo16(md[1536 + oc]), xv = lo16(md[2048 + oc]);
      v4u cp[4] = {};
      for (unsigned spins = 0;; ++spins) {
        CBAR();
#pragma unroll
        for (int i = 0; i < 2; ++i) cp[i] = ldsc(rws, vo, BY_OFF + i * 16384 + jc * 128);
        if (!__any(umax(umax4(cp[0]), umax4(cp[1])) == SENT) || spins > SPIN_CAP) break;
        __builtin_amdgcn_s_sleep(8);
      }
      const f32x4 ys = asf(cp[0]) + asf(cp[1]);
      float y = row_tr(ys[0], ys[1], ys[2], ys[3]);
      y += dpp<ROR8>(y);
      float x1 = rbf(xv + rbf(ga * rbf(y)));
      GAS u16* po = (GAS u16*)out + oc;
      asm volatile("" : "+v"(x1), "+v"(po));
      for (unsigned spins = 0;; ++spins) {
        CBAR();
#pragma unroll
        for (int i = 0; i < 4; ++i) cp[i] = ldsc(rws, vo, CO_OFF + i * 16384 + jc * 128);
        if (!__any(umax(umax(umax4(cp[0]), umax4(cp[1])), umax(umax4(cp[2]), umax4(cp[3]))) == SENT) || spins > SPIN_CAP) break;
      }
      STAMP(16);
      const f32x4 cs = (asf(cp[0]) + asf(cp[1])) + (asf(cp[2]) + asf(cp[3]));
      float os = row_tr(cs[0], cs[1], cs[2], cs[3]);
      os += dpp<ROR8>(os);
      const float o = x1 + rbf(gb * rbf(os));
      if (n < 8) *po = (u16)cvtpk(o, o);
      if (jc == 0 && lane == 0) __hip_atomic_store((gu32*)(ws + EPOCH_OFF), tag, RLX_AGENT);
      STAMP(17);
    }
  }
}

extern "C" void kernel_launch(void* const* d_in, const int* in_sizes, int n_in, void* d_out, int out_size, void* d_ws, size_t ws_size,
                              hipStream_t stream) {
  (void)in_sizes; (void)n_in; (void)out_size; (void)ws_size;
  (void)hipFuncSetAttribute((const void*)fused_decode, hipFuncAttributeMaxDynamicSharedMemorySize, L_BYTES);
  fused_decode<<<NWG, NTHR, L_BYTES, stream>>>((const u16*)d_in[0], (const u16*)d_in[1], (const u16*)d_in[2], (const u16*)d_in[3], (const u16*)d_in[4],
                                               (const u16*)d_in[6], (const u16*)d_in[7], (const u16*)d_in[8], (const u16*)d_in[9], (u16*)d_out,
                                               (unsigned char*)d_ws);
}
```

```cpp
#include <hip/hip_runtime.h>
#include <stdint.h>

#define GAS __attribute__((address_space(1)))
#define LAS __attribute__((address_space(3)))
typedef unsigned v4u __attribute__((ext_vector_type(4)));
typedef unsigned v2u __attribute__((ext_vector_type(2)));
typedef float f32x4 __attribute__((ext_vector_type(4)));
typedef float f32x2 __attribute__((ext_vector_type(2)));
typedef short bf16x8 __attribute__((ext_vector_type(8)));
typedef __bf16 bf16x2 __attribute__((ext_vector_type(2)));
typedef unsigned short u16;
typedef unsigned long long u64;
typedef GAS unsigned gu32;
typedef GAS u64 gu64;
#define RLX_AGENT __ATOMIC_RELAXED, __HIP_MEMORY_SCOPE_AGENT

#define DIST 2
#define DISTE 3
#define KEND 6
#define SIGK 8
#define MWAIT 580
#define QWAIT 980
#define PROUNDS 4
#define PT0 1500
#define PTS 600
#define PTD 250
#define PTAU 57
#define PGAIN 4.0f
#define PBIAS 0.25f
#define C2LEAD 1
#define WPRE 44
#define CODEPF 162

constexpr int D = 512, SK = 65536, DH = 128, FF = 1792;
constexpr int NWG = 256, NW = 8, NTHR = NW * 64;
constexpr int AUX_NT = 2, AUX_SC1 = 16;
constexpr unsigned SENT = 0xFFFFFFFFu;
constexpr unsigned SPIN_CAP = 1u << 16;
constexpr int OOB = 0x40000000;
constexpr int EPOCH_OFF = 0;
constexpr int PRG_OFF = 32768;
constexpr int MODG_OFF = 4096;
constexpr int QG_OFF = 16384;
constexpr int AO_OFF = 65536;
constexpr int BY_OFF = AO_OFF + 256 * 1024;
constexpr int CO_OFF = BY_OFF + 32 * 1024;
constexpr int WS_BYTES = CO_OFF + 28 * 2048;
constexpr int L_W = 0;
constexpr int L_MO = 131072;
constexpr int MOW = 8 * 288 + 32;
constexpr int L_MM = L_MO + 8 * MOW;
constexpr int L_ML = L_MM + 128;
constexpr int L_Q = L_ML + 128;
constexpr int L_ACT = L_Q + 1024;
constexpr int L_RED = L_ACT + 1024;
constexpr int L_ST = L_RED + 4096;
constexpr int L_H1 = L_ST + 64;
constexpr int L_MOD = L_H1 + 1024;
constexpr int L_CTL = L_MOD + 5120;
constexpr int L_BYTES = L_CTL + 32;
static_assert(L_BYTES <= 160 * 1024, "LDS");

__device__ __forceinline__ float lo16(unsigned w) { return __builtin_bit_cast(float, w << 16); }
__device__ __forceinline__ float hi16(unsigned w) { return __builtin_bit_cast(float, w & 0xffff0000u); }
__device__ __forceinline__ unsigned cvtpk(float lo, float hi) {
  return __builtin_bit_cast(unsigned, __builtin_convertvector((f32x2{lo, hi}), bf16x2));
}
#define rbf(x) __builtin_bit_cast(float, __builtin_convertvector((f32x2{zero_, (x)}), bf16x2))
__device__ __forceinline__ f32x4 mfma(v4u a, v4u b, f32x4 c) {
  return __builtin_amdgcn_mfma_f32_16x16x32_bf16(__builtin_bit_cast(bf16x8, a), __builtin_bit_cast(bf16x8, b), c, 0, 0, 0);
}
__device__ __forceinline__ float dot2(unsigned a, unsigned b, float acc) {
  return __builtin_amdgcn_fdot2_f32_bf16(__builtin_bit_cast(bf16x2, a), __builtin_bit_cast(bf16x2, b), acc, false);
}
__device__ __forceinline__ float rcp(float x) { return __builtin_amdgcn_rcpf(x); }
__device__ __forceinline__ float ex2(float x) { return __builtin_amdgcn_exp2f(x); }
__device__ __forceinline__ float silu(float x) { return x * rcp(1.0f + ex2(x * -1.4426950408889634f)); }
template <int CTRL> __device__ __forceinline__ float dpp(float x) {
  return __builtin_bit_cast(float, __builtin_amdgcn_mov_dpp(__builtin_bit_cast(int, x), CTRL, 0xf, 0xf, true));
}
constexpr int XOR1 = 0xB1, XOR2 = 0x4E, HMIR = 0x141, ROR4 = 0x124, ROR8 = 0x128;
__device__ __forceinline__ float sum16(float x) {
  auto s = __builtin_amdgcn_permlane16_swap(__float_as_uint(x), __float_as_uint(x), false, false);
  return __uint_as_float(s[0]) + __uint_as_float(s[1]);
}
__device__ __forceinline__ float sum32(float x) {
  auto s = __builtin_amdgcn_permlane32_swap(__float_as_uint(x), __float_as_uint(x), false, false);
  return __uint_as_float(s[0]) + __uint_as_float(s[1]);
}
__device__ __forceinline__ float max16(float x) {
  auto s = __builtin_amdgcn_permlane16_swap(__float_as_uint(x), __float_as_uint(x), false, false);
  return fmaxf(__uint_as_float(s[0]), __uint_as_float(s[1]));
}
__device__ __forceinline__ float max32(float x) {
  auto s = __builtin_amdgcn_permlane32_swap(__float_as_uint(x), __float_as_uint(x), false, false);
  return fmaxf(__uint_as_float(s[0]), __uint_as_float(s[1]));
}
__device__ __forceinline__ float sum_q(float x) { x += dpp<XOR1>(x); x += dpp<XOR2>(x); return x; }
__device__ __forceinline__ float sum_b23(float x) { x += dpp<ROR4>(x); x += dpp<ROR8>(x); return x; }
__device__ __forceinline__ float sum_row(float x) { return sum_b23(sum_q(x)); }
__device__ __forceinline__ float wave_sum(float x) { return sum32(sum16(sum_row(x))); }
__device__ __forceinline__ float tr16(float a, float b) {
  auto s = __builtin_amdgcn_permlane16_swap(__float_as_uint(a), __float_as_uint(b), false, false);
  return __uint_as_float(s[0]) + __uint_as_float(s[1]);
}
__device__ __forceinline__ float tr32(float a, float b) {
  auto s = __builtin_amdgcn_permlane32_swap(__float_as_uint(a), __float_as_uint(b), false, false);
  return __uint_as_float(s[0]) + __uint_as_float(s[1]);
}
__device__ __forceinline__ float row_tr(float a, float b, float c, float d) { return tr32(tr16(a, b), tr16(c, d)); }
#define LGKM0() asm volatile("s_waitcnt lgkmcnt(0)" ::: "memory")
#define VM0() asm volatile("s_waitcnt vmcnt(0)" ::: "memory")
#define BAR() do { LGKM0(); __builtin_amdgcn_s_barrier(); asm volatile("" ::: "memory"); } while (0)
#define SCHED() __builtin_amdgcn_sched_barrier(0)
#define CBAR() asm volatile("" ::: "memory")
__device__ __forceinline__ unsigned umax(unsigned a, unsigned b) { return a > b ? a : b; }
__device__ __forceinline__ unsigned umax4(v4u v) { return umax(umax(v.x, v.y), umax(v.z, v.w)); }
__device__ __forceinline__ __amdgpu_buffer_rsrc_t mkrs(const void* p, int bytes) { return __builtin_amdgcn_make_buffer_rsrc((void*)p, 0, bytes, 0x00020000); }
__device__ __forceinline__ v4u ldnt(__amdgpu_buffer_rsrc_t r, int vo, int so) { return __builtin_amdgcn_raw_buffer_load_b128(r, vo, so, AUX_NT); }
__device__ __forceinline__ v4u ldsc(__amdgpu_buffer_rsrc_t r, int vo, int so) { return __builtin_amdgcn_raw_buffer_load_b128(r, vo, so, AUX_SC1); }
__device__ __forceinline__ void stsc(v4u v, __amdgpu_buffer_rsrc_t r, int vo, int so) { __builtin_amdgcn_raw_buffer_store_b128(v, r, vo, so, AUX_SC1); }
__device__ __forceinline__ f32x4 asf(v4u v) { return __builtin_bit_cast(f32x4, v); }
__device__ __forceinline__ v4u asu(f32x4 v) { return __builtin_bit_cast(v4u, v); }
#define PLO 0x05040100u
#define PHI 0x07060302u

__global__ void __launch_bounds__(NTHR, 2)
fused_decode(const u16* __restrict__ xin, const u16* __restrict__ kc, const u16* __restrict__ vc, const u16* __restrict__ wq,
             const u16* __restrict__ wo, const u16* __restrict__ w1, const u16* __restrict__ w2, const u16* __restrict__ cin,
             const u16* __restrict__ wmod, u16* __restrict__ out, unsigned char* ws) {
  extern __shared__ __attribute__((aligned(16))) unsigned char smem[];
  unsigned ka0, ka1;
  asm volatile("s_load_dword %0, %2, 0x0\n\ts_load_dword %1, %2, 0x40" : "=&s"(ka0), "=&s"(ka1) : "s"(__builtin_amdgcn_kernarg_segment_ptr()));
  const u64 t_entry = __builtin_amdgcn_s_memrealtime();
  unsigned pf0 = 0;
  if (threadIdx.x < CODEPF) pf0 = *(const GAS unsigned*)((__builtin_amdgcn_s_getpc() & ~127ull) + threadIdx.x * 128);
  LAS unsigned char* L = (LAS unsigned char*)smem;
  volatile LAS unsigned* ctl = (volatile LAS unsigned*)(L + L_CTL);
  const int w_0 = __builtin_amdgcn_readfirstlane(threadIdx.x >> 6);
#define INDICES() \
  int lane = threadIdx.x & 63, w = w_0, b = blockIdx.x; \
  asm volatile("" : "+v"(lane)); asm volatile("" : "+s"(w)); asm volatile("" : "+s"(b)); \
  float zero_ = 0.f; asm volatile("" : "+v"(zero_)); \
  const int g = lane >> 4, n = lane & 15, xg = b & 7, ig = b >> 3; \
  const bool isC2 = ig >= 24, isC3 = ig >= 20 && ig < 24 && xg < 7, isC4 = (ig >> 1) == 8; \
  const int hp = 2 * xg + ((ig >> 2) & 1), cq = ig & 3;         \
  const int j3 = 4 * xg + (ig - 20);                            \
  (void)g; (void)n; (void)isC2; (void)isC3; (void)isC4; (void)hp; (void)cq; (void)j3;
  const __amdgpu_buffer_rsrc_t rws = mkrs(ws, WS_BYTES);
  if (threadIdx.x < 8) ctl[threadIdx.x] = 0u;
  LAS float* red = (LAS float*)(L + L_RED);
#define GCOL (ig < 8 ? 1024 + 256 * xg + 64 * (ig - 4) : 512 * xg + 64 * (ig - 8))
  unsigned tag0 = 0; v4u in8 = {};
  {
    INDICES();
    int rs0 = isC2 ? D * 2 : isC3 ? FF * 2 : ig < 8 ? 6144 : 8192;
    int sz0 = isC2 ? 4096 * D * 2 : D * rs0;
    int so0 = isC2 ? (256 * hp + 32 * w) * (D * 2) + cq * 256 : 64 * w * rs0 + (isC3 ? 64 * j3 : ig < 4 ? 128 * xg + 32 * ig : GCOL) * 2;
    int vo0 = isC2 ? (8 * g) * (D * 2) + n * 16 : ig < 4 ? (4 * (lane >> 2)) * 6144 + (lane & 3) * 16 : (8 * (lane >> 3)) * rs0 + (lane & 7) * 16;
    asm volatile("" : "+s"(rs0)); asm volatile("" : "+s"(sz0)); asm volatile("" : "+s"(so0)); asm volatile("" : "+v"(vo0));
    asm volatile("" ::"s"(xin), "s"(kc), "s"(vc), "s"(wq), "s"(wo), "s"(w1), "s"(w2), "s"(cin), "s"(wmod), "s"(out), "s"(ws) : "memory");
    asm volatile("s_waitcnt lgkmcnt(0)" ::"s"(ka0), "s"(ka1) : "memory");
    BAR();
    const __amdgpu_buffer_rsrc_t r0 = mkrs(isC2 ? wo : isC3 ? w1 : ig < 8 ? wmod : wq, sz0);
    if (w == 0) {
      tag0 = __hip_atomic_load((gu32*)(ws + EPOCH_OFF), RLX_AGENT);
#pragma unroll
      for (int d = 0; d < 4; ++d) in8[d] = ((const GAS unsigned*)(ig < 8 ? cin : xin))[lane + 64 * d];
    }
    if (ig < 4) {
      const int c0 = 128 * xg + 32 * ig;
      v4u R[4];
#pragma unroll
      for (int j = 0; j < 4; ++j) R[j] = ldnt(r0, vo0, so0 + j * 6144);
      const v2u c4 = *(const GAS v2u*)((const GAS u16*)cin + 64 * w + 4 * (lane >> 2));
      stsc(v4u{SENT, SENT, SENT, SENT}, rws, lane < 32 ? lane * 16 : OOB, BY_OFF + ((4 * xg + ig) * 8 + w) * 512);
      SCHED();
      const unsigned a0 = cvtpk(silu(lo16(c4.x)), silu(hi16(c4.x))), a1 = cvtpk(silu(lo16(c4.y)), silu(hi16(c4.y)));
      float acc[8];
#pragma unroll
      for (int d = 0; d < 4; ++d) {
        acc[2 * d] = dot2(__builtin_amdgcn_perm(R[3][d], R[2][d], PLO), a1, dot2(__builtin_amdgcn_perm(R[1][d], R[0][d], PLO), a0, 0.f));
        acc[2 * d + 1] = dot2(__builtin_amdgcn_perm(R[3][d], R[2][d], PHI), a1, dot2(__builtin_amdgcn_perm(R[1][d], R[0][d], PHI), a0, 0.f));
      }
      const float t0 = sum_b23(row_tr(acc[0], acc[2], acc[4], acc[6])), t1 = sum_b23(row_tr(acc[1], acc[3], acc[5], acc[7]));
      *(LAS f32x2*)(red + w * 32 + (n & 3) * 8 + g * 2) = f32x2{t0, t1};
      VM0();
      BAR();
      if (w == 0 && lane < 16) {
        f32x2 t = {0.f, 0.f};
        asm volatile("" : "+v"(tag0));
#pragma unroll
        for (int i = 0; i < NW; ++i) t += *(const LAS f32x2*)(red + i * 32 + 2 * lane);
        __hip_atomic_store((gu64*)(ws + MODG_OFF) + (c0 >> 1) + lane, ((u64)(tag0 + 1u) << 32) | cvtpk(t.x, t.y), RLX_AGENT);
      }
    } else if (ig < 16 || isC2 || isC3) {
      const int ro = lane >> 3, c8 = lane & 7;
      const bool one = !isC3;
      v4u R[2][8];
#pragma unroll
      for (int u = 0; u < 8; ++u) R[0][u] = ldnt(r0, vo0, so0 + u * rs0);
      SCHED();
      const __amdgpu_buffer_rsrc_t r1 = mkrs(w2, one ? 0 : D * FF * 2);
#pragma unroll
      for (int u = 0; u < 8; ++u) R[1][u] = ldnt(r1, (8 * ro) * (D * 2) + c8 * 16, (64 * (j3 & 31) + u) * (D * 2) + w * 128);
      stsc(v4u{SENT, SENT, SENT, SENT}, rws, (ig >= 8 && ig < 16 && lane < 32) ? lane * 16 : OOB, AO_OFF + xg * 32768 + ((ig & 7) * 8 + w) * 512);
      SCHED();
      const int la = isC2 ? (((n >> 1) * 8 + w) * 64 + g * 16 + 8 * (n & 1)) * 16 : (((w * 2 + (ro >> 2)) * 4 + (c8 >> 1)) * 64 + (ro & 3) * 16 + 8 * (c8 & 1)) * 16;
#pragma unroll
      for (int h = 0; h < 2; ++h) {
        if (h && one) break;
#pragma unroll
        for (int e = 0; e < 8; ++e) {
          v4u u;
#pragma unroll
          for (int jj = 0; jj < 4; ++jj) u[jj] = __builtin_amdgcn_perm(R[h][2 * jj + 1][e >> 1], R[h][2 * jj][e >> 1], (e & 1) ? PHI : PLO);
          *(LAS v4u*)(L + L_W + h * 65536 + la + e * 16) = u;
        }
      }
      VM0();
      LGKM0();
      if (lane == 0) __hip_atomic_fetch_add((LAS unsigned*)(L + L_CTL + 16), 1u, __ATOMIC_RELAXED, __HIP_MEMORY_SCOPE_WORKGROUP);
    }
  }
  if (w_0 == 0) {
    INDICES();
    __builtin_amdgcn_s_setprio(3);
    asm volatile("" : "+v"(tag0));
    const unsigned tag = tag0 + 1u;
    if (ig >= 4 && ig < 16) {
      const bool isq = ig >= 8;
      for (unsigned spins = 0; __builtin_amdgcn_readfirstlane(ctl[4]) < 8u && spins < (1u << 22); ++spins) __builtin_amdgcn_s_sleep(1);
      CBAR();
      v4u wf[WPRE];
#pragma unroll
      for (int k = 0; k < WPRE; ++k) { wf[k] = *(const LAS v4u*)(L + L_W + (k * 64 + lane) * 16); }
#pragma unroll
      for (int k = 0; k < WPRE; ++k) asm volatile("" : "+v"(wf[k]));
      float xv[8];
#pragma unroll
      for (int d = 0; d < 4; ++d) { xv[2 * d] = lo16(in8[d]); xv[2 * d + 1] = hi16(in8[d]); }
      if (isq) {
        const float mean = wave_sum(((xv[0] + xv[1]) + (xv[2] + xv[3])) + ((xv[4] + xv[5]) + (xv[6] + xv[7]))) * (1.0f / D);
        float sq = 0.f;
#pragma unroll
        for (int e = 0; e < 8; ++e) sq += (xv[e] - mean) * (xv[e] - mean);
        const float var = rbf(wave_sum(sq) * (1.0f / D));
        const float mb = rbf(mean), isd = rcp(rbf(__builtin_amdgcn_sqrtf(rbf(var + 1e-5f))));
#pragma unroll
        for (int e = 0; e < 8; ++e) xv[e] = rbf(rbf(xv[e] - mb) * isd);
        for (unsigned spins = 0; __builtin_amdgcn_s_memrealtime() - t_entry < MWAIT && spins < 1024; ++spins) __builtin_amdgcn_s_sleep(2);
        const gu64* gs = (const gu64*)(ws + MODG_OFF) + lane;
        u64 v[8];
        for (unsigned spins = 0;; ++spins) {
          bool ok = true;
#pragma unroll
          for (int d = 0; d < 8; ++d) v[d] = __hip_atomic_load((gu64*)gs + 64 * d, RLX_AGENT);
#pragma unroll
          for (int d = 0; d < 8; ++d) ok &= (unsigned)(v[d] >> 32) == tag;
          if (__all(ok) || spins > SPIN_CAP) break;
          __builtin_amdgcn_s_sleep(4);
        }
#pragma unroll
        for (int d = 0; d < 4; ++d) {
          const unsigned sa = (unsigned)v[d], ca = (unsigned)v[4 + d];
          xv[2 * d] = rbf(xv[2 * d] * rbf(1.0f + lo16(ca))) + lo16(sa);
          xv[2 * d + 1] = rbf(xv[2 * d + 1] * rbf(1.0f + hi16(ca))) + hi16(sa);
        }
      } else {
#pragma unroll
        for (int e = 0; e < 8; ++e) xv[e] = silu(xv[e]);
      }
#pragma unroll
      for (int d = 0; d < 4; ++d) ((LAS unsigned*)(L + L_ACT))[lane + 64 * d] = cvtpk(xv[2 * d], xv[2 * d + 1]);
      CBAR();
      f32x4 acc[4] = {};
#pragma unroll
      for (int k = 0; k < 16; ++k) {
        const v4u a = *(const LAS v4u*)(L + L_ACT + k * 64 + g * 16);
#pragma unroll
        for (int t = 0; t < 4; ++t) acc[t] = mfma(k * 4 + t < WPRE ? wf[(k * 4 + t) % WPRE] : *(const LAS v4u*)(L + L_W + ((k * 4 + t) * 64 + lane) * 16), a, acc[t]);
      }
      const f32x4 ts = (n & 4) ? ((n & 2) ? acc[3] : acc[2]) : ((n & 2) ? acc[1] : acc[0]);
      if (n < 8)
        __hip_atomic_store((gu64*)(ws + (isq ? QG_OFF : MODG_OFF)) + (GCOL >> 1) + 8 * (n >> 1) + 2 * g + (n & 1),
                           ((u64)tag << 32) | ((n & 1) ? cvtpk(ts[2], ts[3]) : cvtpk(ts[0], ts[1])), RLX_AGENT);
    }
    for (unsigned spins = 0; __builtin_amdgcn_s_memrealtime() - t_entry < QWAIT && spins < 1024; ++spins) __builtin_amdgcn_s_sleep(4);
    {
      const gu64* gs = (const gu64*)(ws + QG_OFF) + 256 * xg + lane;
      u64 v[4];
      for (unsigned spins = 0;; ++spins) {
        bool ok = true;
#pragma unroll
        for (int d = 0; d < 4; ++d) v[d] = __hip_atomic_load((gu64*)gs + 64 * d, RLX_AGENT);
#pragma unroll
        for (int d = 0; d < 4; ++d) ok &= (unsigned)(v[d] >> 32) == tag;
        if (__all(ok) || spins > SPIN_CAP) break;
        __builtin_amdgcn_s_sleep(8);
      }
#pragma unroll
      for (int d = 0; d < 4; ++d) ((LAS unsigned*)(L + L_Q))[64 * d + lane] = (unsigned)v[d];
      ctl[1] = tag;
      LGKM0();
      ctl[0] = 1u;
    }
    __builtin_amdgcn_s_setprio(0);
    if (isC3 || isC4) {
      const gu64* gm = (const gu64*)(ws + MODG_OFF) + 512 + lane;
      u64 t[16];
      for (unsigned spins = 0;; ++spins) {
        bool ok = true;
#pragma unroll
        for (int d = 0; d < 16; ++d) t[d] = __hip_atomic_load((gu64*)gm + 64 * d, RLX_AGENT);
#pragma unroll
        for (int d = 0; d < 16; ++d) ok &= (unsigned)(t[d] >> 32) == tag;
        if (__all(ok) || spins > SPIN_CAP) break;
      }
#pragma unroll
      for (int d = 0; d < 16; ++d) ((LAS unsigned*)(L + L_MOD))[64 * d + lane] = (unsigned)t[d];
#pragma unroll
      for (int d = 0; d < 4; ++d) ((LAS unsigned*)(L + L_MOD + 4096))[lane + 64 * d] = in8[d];
    }
#pragma unroll
    for (int i = 0; i < 3; ++i) if (lane + 64 * i < MOW / 16) *(LAS f32x4*)(L + L_MO + (lane + 64 * i) * 16) = f32x4{0.f, 0.f, 0.f, 0.f};
    if (lane < 4) { ((LAS float*)(L + L_MM))[lane * 8] = -1e30f; ((LAS float*)(L + L_ML))[lane * 8] = 0.f; }
    const int nt = 64 + (isC2 ? C2LEAD : 0);
#define LEFT() (nt - (int)__builtin_amdgcn_readfirstlane(ctl[2]))
#pragma unroll 1
    for (int rd = 0; rd < PROUNDS; ++rd) {
      const unsigned tp = PT0 + rd * PTS;
      for (unsigned spins = 0; __builtin_amdgcn_s_memrealtime() - t_entry < tp && LEFT() > 8 && spins < 4096; ++spins) __builtin_amdgcn_s_sleep(4);
      const int rem = LEFT();
      if (rem <= 8) break;
      const unsigned fe = (unsigned)__builtin_amdgcn_s_memrealtime() + (unsigned)rem * PTAU;
      if (lane == 0 && (ig & 7) == xg) __hip_atomic_store((gu64*)(ws + PRG_OFF) + rd * 512 + ((ig >> 3) * 8 + xg) * 16, ((u64)tag << 32) | fe, RLX_AGENT);
      for (unsigned spins = 0; __builtin_amdgcn_s_memrealtime() - t_entry < tp + PTD && spins < 4096; ++spins) __builtin_amdgcn_s_sleep(4);
      const u64 pv = __hip_atomic_load((gu64*)(ws + PRG_OFF) + rd * 512 + (lane & 31) * 16, RLX_AGENT);
      const bool ok = (unsigned)(pv >> 32) == tag;
      const float cnt = wave_sum(ok ? 1.f : 0.f), sum = wave_sum(ok ? (float)(int)(fe - (unsigned)pv) : 0.f);
      const float lag = sum * rcp(fmaxf(cnt, 1.f)) * (1.0f / PTAU);
      ctl[3] = lag > PBIAS ? ctl[2] + 2u + (unsigned)(PGAIN * (lag - PBIAS)) : 0u;
    }
  } else {
    INDICES();
    constexpr float SCALE = 0.088388347648318440f, C2 = 1.4426950408889634f;
    constexpr int nt = 64;
    const int t0 = 64 * ig;
    const __amdgpu_buffer_rsrc_t rk = mkrs(kc + ((size_t)xg * SK + (size_t)t0 * 32) * DH, nt * 8192);
    const __amdgpu_buffer_rsrc_t rv = mkrs(vc + ((size_t)xg * SK + (size_t)t0 * 32) * DH, nt * 8192);
    int koff = (4 * (n & 3) + ((n >> 2) & 1)) * 256 + (n >> 3) * 64 + 16 * g, voff = lane * 16;
    v4u kf[3][8], vr[3][8];
    int id = w - 1;
#define ISSUE(d, dist) { \
      for (unsigned spins = 0; spins < (1u << 22); ++spins) { \
        const u64 c_ = *(volatile LAS u64*)(L + L_CTL + 8); \
        if ((int)__builtin_amdgcn_readfirstlane((unsigned)c_) + (dist) + (id < (int)__builtin_amdgcn_readfirstlane((unsigned)(c_ >> 32)) ? 1 : 0) > id) break; \
        __builtin_amdgcn_s_sleep(1); } \
      const int so_ = id * 8192; asm volatile("" : "+v"(koff), "+v"(voff) :: "memory"); \
      _Pragma("unroll") for (int u = 0; u < 8; ++u) kf[d][u] = ldnt(rk, koff + ((u >> 1) & 1) * 512 + (u & 1) * 128, so_ + (u >> 2) * 4096); \
      _Pragma("unroll") for (int u = 0; u < 8; ++u) vr[d][u] = ldnt(rv, voff + (u & 3) * 1024, so_ + (u >> 2) * 4096); \
      SCHED(); }
#define SIGNAL() { \
      SCHED(); \
      asm volatile("s_waitcnt vmcnt(%0)" ::"i"(SIGK) : "memory"); \
      if (lane == 0) __hip_atomic_fetch_add((LAS unsigned*)(L + L_CTL + 8), 1u, __ATOMIC_RELAXED, __HIP_MEMORY_SCOPE_WORKGROUP); \
      SCHED(); \
      id += 7; }
#define QK(d) { \
      f32x4 sq[4]; \
      _Pragma("unroll") for (int t = 0; t < 4; ++t) sq[t] = mfma(kf[d][2 * t + 1], qf[1], mfma(kf[d][2 * t], qf[0], f32x4{0.f, 0.f, 0.f, 0.f})); \
      float sc[2][4]; \
      _Pragma("unroll") for (int j = 0; j < 8; ++j) { \
        auto x_ = __builtin_amdgcn_permlane32_swap(__float_as_uint(sq[2 * (j >> 2)][j & 3]), __float_as_uint(sq[2 * (j >> 2) + 1][j & 3]), false, false); \
        sc[j >> 2][j & 3] = rbf(rbf(__uint_as_float(x_[0]) + dpp<ROR4>(__uint_as_float(x_[1]))) * SCALE); } \
      float tm = fmaxf(fmaxf(fmaxf(sc[0][0], sc[0][1]), fmaxf(sc[0][2], sc[0][3])), fmaxf(fmaxf(sc[1][0], sc[1][1]), fmaxf(sc[1][2], sc[1][3]))); \
      tm = max32(max16(tm)); \
      const float mn = fmaxf(m, tm * C2); \
      const float alpha = ex2(m - mn); \
      m = mn; \
      float p[8]; \
      _Pragma("unroll") for (int j = 0; j < 8; ++j) p[j] = ex2(sc[j >> 2][j & 3] * C2 - mn); \
      l = l * alpha + (((p[0] + p[1]) + (p[2] + p[3])) + ((p[4] + p[5]) + (p[6] + p[7]))); \
      pf = v4u{cvtpk(p[0], p[1]), cvtpk(p[2], p[3]), cvtpk(p[4], p[5]), cvtpk(p[6], p[7])}; \
      _Pragma("unroll") for (int e = 0; e < 8; ++e) acc[e] *= alpha; }
#define PV(d) { \
      _Pragma("unroll") for (int e = 0; e < 8; ++e) { \
        v4u bf; \
        _Pragma("unroll") for (int jj = 0; jj < 4; ++jj) \
          bf[jj] = __builtin_amdgcn_perm(vr[d][2 * jj + 1][e >> 1], vr[d][2 * jj][e >> 1], (e & 1) ? PHI : PLO); \
        acc[e] = mfma(bf, pf, acc[e]); } \
      SCHED(); }
    ISSUE(2, DIST) SIGNAL() ISSUE(1, DIST) SIGNAL() ISSUE(0, DIST) SIGNAL()
    for (unsigned spins = 0; __builtin_amdgcn_readfirstlane(ctl[0]) == 0u && spins < (1u << 22); ++spins) __builtin_amdgcn_s_sleep(1);
    CBAR();
    v4u qf[2], pf;
#pragma unroll
    for (int s = 0; s < 2; ++s) qf[s] = *(const LAS v4u*)(L + L_Q + (n & 3) * 256 + 128 * s + 64 * ((n >> 2) & 1) + 16 * g);
    f32x4 acc[8];
#pragma unroll
    for (int e = 0; e < 8; ++e) acc[e] = f32x4{0.f, 0.f, 0.f, 0.f};
    float m = -1e30f, l = 0.f;
    int a_mo = L_MO + w * MOW + (n * 4 + g) * 16;
    asm volatile("" : "+v"(a_mo));
    QK(2) PV(2) QK(1) PV(1)
#pragma unroll 1
    for (;;) {
      QK(0) PV(0)
      if (id >= nt) break;
      ISSUE(0, (id >= nt - KEND ? DISTE : DIST))
      SIGNAL()
    }
    l = sum32(sum16(l));
    if (n < 4) {
#pragma unroll
      for (int e = 0; e < 8; ++e) *(LAS f32x4*)(L + a_mo + 288 * e) = acc[e];
      ((LAS float*)(L + L_MM))[n * 8 + w] = m; ((LAS float*)(L + L_ML))[n * 8 + w] = l;
    }
  }
  asm volatile("" ::"v"(pf0));
  {
    INDICES();
    const unsigned tag = ctl[1];
    {
      const int r = w >> 1, sub = lane & 3;
      int a_m = L_MM + r * 32 + sub * 8, a_o = L_MO + 2 * sub * MOW + ((lane >> 2) & 7) * 288 + (r * 4 + (w & 1) * 2 + (lane >> 5)) * 16,
          sv = sub == 0 ? AO_OFF + (lane >> 2) * 8 : OOB, sh = (lane >> 2) * 2;
      asm volatile("" : "+v"(a_m), "+v"(a_o), "+v"(sv), "+v"(sh));
      BAR();
      const f32x2 mo = *(const LAS f32x2*)(L + a_m), lo = *(const LAS f32x2*)(L + a_m + 128);
      const f32x4 oa = *(const LAS f32x4*)(L + a_o), ob = *(const LAS f32x4*)(L + a_o + MOW);
      float M = fmaxf(mo.x, mo.y);
      M = fmaxf(M, dpp<XOR1>(M)); M = fmaxf(M, dpp<XOR2>(M));
      const float wa = ex2(mo.x - M), wb = ex2(mo.y - M);
      const float Ls = sum_q(wa * lo.x + wb * lo.y);
      const float inv = rcp(Ls);
      const unsigned lse = __float_as_uint(M + __builtin_amdgcn_logf(Ls)) >> sh;
      f32x4 o = wa * oa + wb * ob;
#pragma unroll
      for (int e = 0; e < 4; ++e) o[e] = sum_q(o[e]) * inv;
      v2u pk = {(cvtpk(o[0], o[1]) & ~1u) | (lse & 1u), (cvtpk(o[2], o[3]) & ~1u) | ((lse >> 1) & 1u)};
      __builtin_amdgcn_raw_buffer_store_b64(pk, rws, sv, (xg * 32 + ig) * 1024 + w * 128, AUX_SC1);
    }
    if (isC2) {
      v4u wf[16];
#pragma unroll
      for (int k = 0; k < 16; ++k) wf[k] = *(const LAS v4u*)(L + L_W + (((w & 3) * 16 + k) * 64 + lane) * 16);
      const int dc = lane & 7;
      int aw = L_ACT + (w & 1) * 512 + ((w >> 1) * 64 + 32 * (dc >> 2) + 8 * g + 2 * (dc & 3)) * 2, ar = L_ACT + ((n >> 1) & 1) * 512 + g * 16, sv = n < 2 ? n * 64 + g * 16 : OOB,
          vo = (lane >> 3) * 1024 + dc * 16, sh = 4 * dc;
      asm volatile("" : "+v"(aw), "+v"(ar), "+v"(sv), "+v"(vo), "+v"(sh));
      v4u o4[2] = {};
      for (unsigned spins = 0;; ++spins) {
        CBAR();
#pragma unroll
        for (int i = 0; i < 2; ++i) o4[i] = ldsc(rws, vo, AO_OFF + xg * 32768 + (w & 1) * 16384 + i * 8192 + (hp & 1) * 512 + (w >> 1) * 128);
        if (!__any(umax(umax4(o4[0]), umax4(o4[1])) == SENT) || spins > SPIN_CAP) break;
        __builtin_amdgcn_s_sleep(2);
      }
      float ls[2];
#pragma unroll
      for (int i = 0; i < 2; ++i) {
        int t = (int)((((o4[i].x & 1u) | ((o4[i].y & 1u) << 1)) | (((o4[i].z & 1u) << 2) | ((o4[i].w & 1u) << 3))) << sh);
        t |= __builtin_amdgcn_mov_dpp(t, XOR1, 0xf, 0xf, true); t |= __builtin_amdgcn_mov_dpp(t, XOR2, 0xf, 0xf, true); t |= __builtin_amdgcn_mov_dpp(t, HMIR, 0xf, 0xf, true);
        ls[i] = __builtin_bit_cast(float, t);
      }
      float M = fmaxf(ls[0], ls[1]);
      M = fmaxf(M, dpp<ROR8>(M)); M = max32(max16(M));
      const float wa = ex2(ls[0] - M), wb = ex2(ls[1] - M);
      float S = wa + wb;
      S += dpp<ROR8>(S); S = sum32(sum16(S));
      float o[8];
#pragma unroll
      for (int d = 0; d < 4; ++d) { o[2 * d] = wa * lo16(o4[0][d]) + wb * lo16(o4[1][d]); o[2 * d + 1] = wa * hi16(o4[0][d]) + wb * hi16(o4[1][d]); }
      float t0 = row_tr(o[0], o[1], o[2], o[3]), t1 = row_tr(o[4], o[5], o[6], o[7]);
      t0 += dpp<ROR8>(t0); t1 += dpp<ROR8>(t1);
      *(LAS unsigned*)(L + aw) = cvtpk(t0, t1);
      *(LAS f32x2*)(L + L_ST + w * 8) = f32x2{M, S};
      BAR();
      if (w < 4) {
      f32x4 y[4] = {};
#pragma unroll
      for (int k = 0; k < 4; ++k) {
        const v4u a0 = *(const LAS v4u*)(L + ar + k * 64), a1 = *(const LAS v4u*)(L + ar + (4 + k) * 64);
        y[0] = mfma(wf[k], a0, y[0]); y[1] = mfma(wf[4 + k], a1, y[1]);
        y[2] = mfma(wf[8 + k], a0, y[2]); y[3] = mfma(wf[12 + k], a1, y[3]);
      }
      float f[2];
#pragma unroll
      for (int h = 0; h < 2; ++h) {
        const f32x4 t = *(const LAS f32x4*)(L + L_ST + h * 32);
        const float Mh = fmaxf(t[0], t[2]), ea = ex2(t[0] - Mh), eb = ex2(t[2] - Mh);
        f[h] = ((n & 2) ? eb : ea) * rcp(ea * t[1] + eb * t[3]);
      }
      f32x4 ys = f[0] * ((n & 1) ? y[2] : y[0]) + f[1] * ((n & 1) ? y[3] : y[1]);
#pragma unroll
      for (int e = 0; e < 4; ++e) ys[e] += dpp<XOR2>(ys[e]);
      stsc(asu(ys), rws, sv, BY_OFF + hp * 2048 + cq * 512 + w * 128);
      }
    } else if (isC3) {
      v4u f1[8], f2[8];
#pragma unroll
      for (int k = 0; k < 8; ++k) f1[k] = *(const LAS v4u*)(L + L_W + ((w * 8 + k) * 64 + lane) * 16);
#pragma unroll
      for (int k = 0; k < 8; ++k) f2[k] = *(const LAS v4u*)(L + L_W + 65536 + ((w * 8 + k) * 64 + lane) * 16);
      const int col = 64 * w + 4 * n + g;
      const LAS u16* md = (const LAS u16*)(L + L_MOD);
      float ga = lo16(md[col]), cb = rbf(1.0f + lo16(md[1024 + col])), xv = lo16(md[2048 + col]);
      int a_st = L_ST + w * 8 + (lane >> 5) * 4, a_hw = L_ACT + col * 2, a_hr = L_ACT + w * 128 + g * 16, a_rw = L_RED + w * 256 + (n & 3) * 64 + g * 16,
          a_rr = L_RED + lane * 4, a_1w = L_H1 + w * 128 + lane * 2, a_1r = L_H1 + w * 128 + g * 16, sv = n < 4 ? n * 64 + g * 16 : OOB;
      const f32x4 z = {0.f, 0.f, 0.f, 0.f};
#define W1_PARTIAL(src) { \
        const v4u a0 = *(const LAS v4u*)(src), a1 = *(const LAS v4u*)((src) + 64); \
        f32x4 t4[4]; \
        _Pragma("unroll") for (int k = 0; k < 4; ++k) t4[k] = mfma(f1[4 + k], a1, mfma(f1[k], a0, z)); \
        ts = (n & 2) ? ((n & 1) ? t4[3] : t4[2]) : ((n & 1) ? t4[1] : t4[0]); }
#define RED_SUM(rr) (((rr[0] + rr[64]) + (rr[128] + rr[192])) + ((rr[256] + rr[320]) + (rr[384] + rr[448])))
      f32x4 ts;
      *(LAS u16*)(L + a_hw) = (u16)(__float_as_uint(cb) >> 16);
      CBAR();
      W1_PARTIAL(L + ((n & 4) ? L_MOD + 1024 + w * 128 + g * 16 : a_hr))
      *(LAS f32x4*)(L + a_rw + ((n & 4) ? 2048 : 0)) = ts;
      *(LAS float*)(L + a_st) = wave_sum(xv);
      BAR();
      float m0;
      {
        const f32x4 t = (*(const LAS f32x4*)(L + L_ST) + *(const LAS f32x4*)(L + L_ST + 16)) + (*(const LAS f32x4*)(L + L_ST + 32) + *(const LAS f32x4*)(L + L_ST + 48));
        m0 = (t[0] + t[2]) * (1.0f / D);
      }
      float cw = RED_SUM(((const LAS float*)(L + a_rr))), sw = RED_SUM(((const LAS float*)(L + a_rr + 2048)));
      asm volatile("" : "+v"(ga), "+v"(cb), "+v"(xv), "+v"(cw), "+v"(sw), "+v"(m0), "+v"(a_st), "+v"(a_hw), "+v"(a_hr), "+v"(a_rw), "+v"(a_rr), "+v"(a_1w), "+v"(a_1r), "+v"(sv));
      BAR();
      v4u yp[4] = {};
      for (unsigned spins = 0;; ++spins) {
        CBAR();
#pragma unroll
        for (int i = 0; i < 4; ++i) yp[i] = ldsc(rws, g * 2048 + n * 16, BY_OFF + i * 8192 + w * 256);
        if (!__any(umax(umax(umax4(yp[0]), umax4(yp[1])), umax(umax4(yp[2]), umax4(yp[3]))) == SENT) || spins > SPIN_CAP) break;
        __builtin_amdgcn_s_sleep(2);
      }
      const f32x4 ys = (asf(yp[0]) + asf(yp[1])) + (asf(yp[2]) + asf(yp[3]));
      const float x1 = rbf(xv + rbf(ga * rbf(row_tr(ys[0], ys[1], ys[2], ys[3])))) - m0;
      *(LAS u16*)(L + a_hw) = (u16)cvtpk(x1 * cb, x1);
      CBAR();
      {
        const v4u a0 = *(const LAS v4u*)(L + a_hr), a1 = *(const LAS v4u*)(L + a_hr + 64);
        SCHED();
        const float st = sum_row(sum16(tr32(x1, x1 * x1)));
        f32x4 t4[4];
#pragma unroll
        for (int k = 0; k < 4; ++k) t4[k] = mfma(f1[4 + k], a1, mfma(f1[k], a0, z));
        *(LAS float*)(L + a_st) = st;
        SCHED();
        ts = (n & 2) ? ((n & 1) ? t4[3] : t4[2]) : ((n & 1) ? t4[1] : t4[0]);
      }
      *(LAS f32x4*)(L + a_rw) = ts;
      BAR();
      {
        f32x4 s4[4]; float r8[8];
#pragma unroll
        for (int i = 0; i < 4; ++i) s4[i] = *(const LAS f32x4*)(L + L_ST + 16 * i);
#pragma unroll
        for (int i = 0; i < 8; ++i) r8[i] = *(const LAS float*)(L + a_rr + 256 * i);
        SCHED();
        const f32x4 t = (s4[0] + s4[1]) + (s4[2] + s4[3]);
        const float mean = (t[0] + t[2]) * (1.0f / D);
        const float var = rbf(fmaxf((t[1] + t[3]) * (1.0f / D) - mean * mean, 0.f));
        const float isd = rcp(rbf(__builtin_amdgcn_sqrtf(rbf(var + 1e-5f))));
        const float hv = silu(rbf(((((r8[0] + r8[1]) + (r8[2] + r8[3])) + ((r8[4] + r8[5]) + (r8[6] + r8[7]))) - (rbf(mean + m0) - m0) * cw) * isd + sw));
        *(LAS u16*)(L + a_1w) = (u16)cvtpk(hv, hv);
        CBAR();
        const v4u a0 = *(const LAS v4u*)(L + a_1r), a1 = *(const LAS v4u*)(L + a_1r + 64);
        f32x4 t4[4];
#pragma unroll
        for (int k = 0; k < 4; ++k) t4[k] = mfma(f2[4 + k], a1, mfma(f2[k], a0, z));
        ts = (n & 2) ? ((n & 1) ? t4[3] : t4[2]) : ((n & 1) ? t4[1] : t4[0]);
        stsc(asu(ts), rws, sv, CO_OFF + j3 * 2048 + w * 256);
      }
    } else if (isC4 && w == 0) {
      const int jc = 2 * xg + (ig & 1), oc = 32 * jc + 4 * (lane & 7) + g, vo = (lane >> 3) * 2048 + (lane & 7) * 16;
      const LAS u16* md = (const LAS u16*)(L + L_MOD);
      const float ga = lo16(md[oc]), gb = lo16(md[1536 + oc]), xv = lo16(md[2048 + oc]);
      v4u cp[4] = {};
      for (unsigned spins = 0;; ++spins) {
        CBAR();
#pragma unroll
        for (int i = 0; i < 2; ++i) cp[i] = ldsc(rws, vo, BY_OFF + i * 16384 + jc * 128);
        if (!__any(umax(umax4(cp[0]), umax4(cp[1])) == SENT) || spins > SPIN_CAP) break;
        __builtin_amdgcn_s_sleep(8);
      }
      const f32x4 ys = asf(cp[0]) + asf(cp[1]);
      float y = row_tr(ys[0], ys[1], ys[2], ys[3]);
      y += dpp<ROR8>(y);
      float x1 = rbf(xv + rbf(ga * rbf(y)));
      GAS u16* po = (GAS u16*)out + oc;
      if (jc == 0 && lane == 0) __hip_atomic_store((gu32*)(ws + EPOCH_OFF), tag, RLX_AGENT);
      asm volatile("" : "+v"(x1), "+v"(po));
      for (unsigned spins = 0;; ++spins) {
        CBAR();
#pragma unroll
        for (int i = 0; i < 4; ++i) cp[i] = ldsc(rws, vo, CO_OFF + i * 16384 + jc * 128);
        if (!__any(umax(umax(umax4(cp[0]), umax4(cp[1])), umax(umax4(cp[2]), umax4(cp[3]))) == SENT) || spins > SPIN_CAP) break;
      }
      const f32x4 cs = (asf(cp[0]) + asf(cp[1])) + (asf(cp[2]) + asf(cp[3]));
      float os = row_tr(cs[0], cs[1], cs[2], cs[3]);
      os += dpp<ROR8>(os);
      const float o = x1 + rbf(gb * rbf(os));
      if (n < 8) *po = (u16)cvtpk(o, o);
    }
  }
}

extern "C" void kernel_launch(void* const* d_in, const int* in_sizes, int n_in, void* d_out, int out_size, void* d_ws, size_t ws_size,
                              hipStream_t stream) {
  (void)in_sizes; (void)n_in; (void)out_size; (void)ws_size;
  (void)hipFuncSetAttribute((const void*)fused_decode, hipFuncAttributeMaxDynamicSharedMemorySize, L_BYTES);
  fused_decode<<<NWG, NTHR, L_BYTES, stream>>>((const u16*)d_in[0], (const u16*)d_in[1], (const u16*)d_in[2], (const u16*)d_in[3], (const u16*)d_in[4],
                                               (const u16*)d_in[6], (const u16*)d_in[7], (const u16*)d_in[8], (const u16*)d_in[9], (u16*)d_out,
                                               (unsigned char*)d_ws);
}
```
